```python
import torch

def _softmax(x, axis=-1):
    return torch.softmax(x, dim=axis)
def _gelu(x):
    return torch.nn.functional.gelu(x, approximate="tanh")
def _silu(x):
    return torch.nn.functional.silu(x)
def _sigmoid(x):
    return torch.sigmoid(x)
def _layernorm(x, axis=-1, eps=1e-5):
    m = x.mean(dim=axis, keepdim=True)
    v = x.var(dim=axis, keepdim=True, unbiased=False)
    return (x - m) / torch.sqrt(v + eps)
def _scatter_add(tgt, idx, src, axis=0):
    return tgt.clone().index_add_(axis, idx, src)
def _segment_sum(src, seg, n):
    out = torch.zeros((n,) + src.shape[1:], dtype=src.dtype)
    return out.index_add_(0, seg, src)
def _pad(x, pw):
    flat = tuple(v for pair in reversed(pw) for v in pair)
    return torch.nn.functional.pad(x, flat)

kernel_name = 'concat_heads__gqa_block_cached__mha_block_cached__near_ridge'

def setup_inputs():

    _w = (lambda a: (lambda t: t.to(torch.bfloat16) if t.is_floating_point() else t)(torch.as_tensor(a)))
    def _A_setup():
        B, Sq, D = 16, 64, 512
        Hq, Hkv, Sk, Dh = 32, 32, 64, 128
        assert Hq % Hkv == 0, (Hq, Hkv)
        return {
            "x": _w((lambda *s: torch.randn(*s, dtype=torch.bfloat16) * 0.8)(B, Sq, D)),
            "k_cache": _w((lambda *s: torch.randn(*s, dtype=torch.bfloat16) * 0.8)(B, Hkv, Sk, Dh)),
            "v_cache": _w((lambda *s: torch.randn(*s, dtype=torch.bfloat16) * 0.05)(B, Hkv, Sk, Dh)),
            "wq": _w((lambda *s: torch.randn(*s, dtype=torch.bfloat16) * 0.8)(D, Hq * Dh) * (D ** -0.5)),
            "wo": _w((lambda *s: torch.randn(*s, dtype=torch.bfloat16) * 0.05)(Hq * Dh, D) * ((Hq * Dh) ** -0.5)),
        }
    _A_in = _A_setup()

    def _B_setup():
        B, Sq, D = 16, 64, 512
        H, Sk, Dh = 32, 64, 128
        return {
            "x": _w((lambda *s: torch.randn(*s, dtype=torch.bfloat16) * 0.8)(B, Sq, D)),
            "k_cache": _w((lambda *s: torch.randn(*s, dtype=torch.bfloat16) * 0.8)(B, H, Sk, Dh)),
            "v_cache": _w((lambda *s: torch.randn(*s, dtype=torch.bfloat16) * 0.05)(B, H, Sk, Dh)),
            "wq": _w((lambda *s: torch.randn(*s, dtype=torch.bfloat16) * 0.8)(D, H * Dh) * (D ** -0.5)),
            "wo": _w((lambda *s: torch.randn(*s, dtype=torch.bfloat16) * 0.05)(H * Dh, D) * ((H * Dh) ** -0.5)),
        }
    _B_in = _B_setup()

    return {
        "A_x": _A_in["x"],
        "A_k_cache": _A_in["k_cache"],
        "A_v_cache": _A_in["v_cache"],
        "A_wq": _A_in["wq"],
        "A_wo": _A_in["wo"],
        "B_x": _B_in["x"],
        "B_k_cache": _B_in["k_cache"],
        "B_v_cache": _B_in["v_cache"],
        "B_wq": _B_in["wq"],
        "B_wo": _B_in["wo"],
    }

def reference(A_x, A_k_cache, A_v_cache, A_wq, A_wo, B_x, B_k_cache, B_v_cache, B_wq, B_wo):
    def _A_ref(x, k_cache, v_cache, wq, wo):
        B, Sq, D = x.shape
        Hq, Dh = 32, 128
        Hkv = k_cache.shape[1]
        rep = Hq // Hkv
        q = (lambda x, *d: x.permute(*d))((x @ wq).reshape(B, Sq, Hq, Dh), 0, 2, 1, 3)
        k = (lambda x, n, axis: torch.repeat_interleave(x, n, dim=axis))(k_cache, rep, axis=1)
        v = (lambda x, n, axis: torch.repeat_interleave(x, n, dim=axis))(v_cache, rep, axis=1)
        scores = torch.einsum("bhqd,bhkd->bhqk", q, k) * (Dh ** -0.5)
        p = _softmax(scores, axis=-1)
        o = torch.einsum("bhqk,bhkd->bhqd", p, v)
        return (lambda x, *d: x.permute(*d))(o, 0, 2, 1, 3).reshape(B, Sq, Hq * Dh) @ wo

    def _B_ref(x, k_cache, v_cache, wq, wo):
        B, Sq, D = x.shape
        H, Dh = 32, 128
        q = (lambda x, *d: x.permute(*d))((x @ wq).reshape(B, Sq, H, Dh), 0, 2, 1, 3)
        scores = torch.einsum("bhqd,bhkd->bhqk", q, k_cache) * (Dh ** -0.5)
        p = _softmax(scores, axis=-1)
        o = torch.einsum("bhqk,bhkd->bhqd", p, v_cache)
        return (lambda x, *d: x.permute(*d))(o, 0, 2, 1, 3).reshape(B, Sq, H * Dh) @ wo

    a = _A_ref(A_x, A_k_cache, A_v_cache, A_wq, A_wo)
    b = _B_ref(B_x, B_k_cache, B_v_cache, B_wq, B_wo)
    return torch.concatenate([a, b], axis=-1)
```

```cpp
#include <hip/hip_runtime.h>
#include <hip/hip_bf16.h>
#include <stdint.h>
typedef float f32x4 __attribute__((ext_vector_type(4)));
typedef __bf16 bf16x8 __attribute__((ext_vector_type(8)));
typedef __bf16 bf16x2 __attribute__((ext_vector_type(2)));
typedef float f32x2 __attribute__((ext_vector_type(2)));
typedef unsigned u32x4 __attribute__((ext_vector_type(4)));
#define DI __device__ __forceinline__
DI unsigned cvtpk(float lo, float hi) { f32x2 v = {lo, hi}; bf16x2 b = __builtin_convertvector(v, bf16x2); return __builtin_bit_cast(unsigned, b); }
DI f32x4 mfma(u32x4 a, u32x4 b, f32x4 c) { return __builtin_amdgcn_mfma_f32_16x16x32_bf16(__builtin_bit_cast(bf16x8, a), __builtin_bit_cast(bf16x8, b), c, 0, 0, 0); }
DI u32x4 tfrag(const u32x4 (&raw)[8], int i) {
  u32x4 f;
#pragma unroll
  for (int u = 0; u < 4; ++u) f[u] = __builtin_amdgcn_perm(raw[2*u+1][i>>1], raw[2*u][i>>1], (i&1) ? 0x07060302u : 0x05040100u);
  return f;
}
struct Args { const uint16_t* x[2]; const uint16_t* k[2]; const uint16_t* v[2]; const uint16_t* wq[2]; const uint16_t* wo[2]; float* part; uint16_t* out; };

__global__ __launch_bounds__(256, 1) void k_main(Args a) {
  __shared__ u32x4 lds[4096];
  const int t = threadIdx.x, l = t & 63, w = t >> 6, r = l & 15, q = l >> 4;
  const int bid = blockIdx.x, xcd = bid & 7, idx = bid >> 3, br = xcd >> 2, hg = (xcd & 3) * 2 + (idx & 1), b = idx >> 1;
  const int h = hg * 4 + w;
  const uint16_t* x = a.x[br] + (size_t)b * 64 * 512;
  for (int c = w; c < 64; c += 4) { int ks = c >> 2, mt = c & 3;
    lds[c * 64 + l] = *(const u32x4*)(x + (16 * mt + r) * 512 + 32 * ks + 8 * q); }
  __syncthreads();
  const int col0 = 32 * (r & 3) + 8 * (r >> 2);
  f32x4 acc[8][4];
#pragma unroll
  for (int i = 0; i < 8; ++i)
#pragma unroll
    for (int m = 0; m < 4; ++m) acc[i][m] = f32x4{0, 0, 0, 0};
  const uint16_t* wq = a.wq[br] + h * 128 + col0 + (size_t)(8 * q) * 4096;
  for (int ks = 0; ks < 16; ++ks) {
    u32x4 raw[8], bf[4];
#pragma unroll
    for (int j = 0; j < 8; ++j) raw[j] = *(const u32x4*)(wq + (size_t)(32 * ks + j) * 4096);
#pragma unroll
    for (int m = 0; m < 4; ++m) bf[m] = lds[(ks * 4 + m) * 64 + l];
#pragma unroll
    for (int i = 0; i < 8; ++i) { u32x4 af = tfrag(raw, i);
#pragma unroll
      for (int m = 0; m < 4; ++m) acc[i][m] = mfma(af, bf[m], acc[i][m]); }
  }
  const uint16_t* kp = a.k[br] + ((size_t)(b * 32 + h) * 64) * 128;
  const uint16_t* vp = a.v[br] + ((size_t)(b * 32 + h) * 64) * 128;
  f32x4 s[4][4];
#pragma unroll
  for (int kt = 0; kt < 4; ++kt)
#pragma unroll
    for (int m = 0; m < 4; ++m) s[kt][m] = f32x4{0, 0, 0, 0};
#pragma unroll
  for (int st = 0; st < 4; ++st) {
    u32x4 bq[4];
#pragma unroll
    for (int m = 0; m < 4; ++m)
#pragma unroll
      for (int u = 0; u < 4; ++u) bq[m][u] = cvtpk(acc[2*u][m][st], acc[2*u+1][m][st]);
#pragma unroll
    for (int kt = 0; kt < 4; ++kt) { u32x4 kf = *(const u32x4*)(kp + (16 * kt + r) * 128 + 32 * st + 8 * q);
#pragma unroll
      for (int m = 0; m < 4; ++m) s[kt][m] = mfma(kf, bq[m], s[kt][m]); }
  }
  const float c = 0.08838834764831845f * 1.4426950408889634f;
  u32x4 pf[2][4];
#pragma unroll
  for (int m = 0; m < 4; ++m) {
    float mx = -INFINITY;
#pragma unroll
    for (int kt = 0; kt < 4; ++kt)
#pragma unroll
      for (int e = 0; e < 4; ++e) mx = fmaxf(mx, s[kt][m][e]);
    mx = fmaxf(mx, __shfl_xor(mx, 16)); mx = fmaxf(mx, __shfl_xor(mx, 32));
    float sum = 0;
#pragma unroll
    for (int kt = 0; kt < 4; ++kt)
#pragma unroll
      for (int e = 0; e < 4; ++e) { float p = __builtin_amdgcn_exp2f((s[kt][m][e] - mx) * c); s[kt][m][e] = p; sum += p; }
    sum += __shfl_xor(sum, 16); sum += __shfl_xor(sum, 32);
    float inv = 1.0f / sum;
#pragma unroll
    for (int s2 = 0; s2 < 2; ++s2)
#pragma unroll
      for (int u = 0; u < 4; ++u) pf[s2][m][u] = cvtpk(s[2*s2 + (u>>1)][m][2*(u&1)] * inv, s[2*s2 + (u>>1)][m][2*(u&1)+1] * inv);
  }
#pragma unroll
  for (int i = 0; i < 8; ++i)
#pragma unroll
    for (int m = 0; m < 4; ++m) acc[i][m] = f32x4{0, 0, 0, 0};
#pragma unroll
  for (int s2 = 0; s2 < 2; ++s2) {
    u32x4 raw[8];
#pragma unroll
    for (int j = 0; j < 8; ++j) raw[j] = *(const u32x4*)(vp + (32 * s2 + 16 * (j >> 2) + 4 * q + (j & 3)) * 128 + col0);
#pragma unroll
    for (int i = 0; i < 8; ++i) { u32x4 af = tfrag(raw, i);
#pragma unroll
      for (int m = 0; m < 4; ++m) acc[i][m] = mfma(af, pf[s2][m], acc[i][m]); }
  }
  __syncthreads();
#pragma unroll
  for (int rho = 0; rho < 4; ++rho)
#pragma unroll
    for (int m = 0; m < 4; ++m) { u32x4 o;
#pragma unroll
      for (int u = 0; u < 4; ++u) o[u] = cvtpk(acc[2*u][m][rho], acc[2*u+1][m][rho]);
      lds[((w * 4 + rho) * 4 + m) * 64 + l] = o; }
  __syncthreads();
#pragma unroll
  for (int i = 0; i < 8; ++i)
#pragma unroll
    for (int m = 0; m < 4; ++m) acc[i][m] = f32x4{0, 0, 0, 0};
  const uint16_t* wo = a.wo[br] + (size_t)(hg * 512 + 8 * q) * 512 + w * 128 + col0;
  for (int ks = 0; ks < 16; ++ks) {
    u32x4 raw[8], bf[4];
#pragma unroll
    for (int j = 0; j < 8; ++j) raw[j] = *(const u32x4*)(wo + (size_t)(32 * ks + j) * 512);
#pragma unroll
    for (int m = 0; m < 4; ++m) bf[m] = lds[(ks * 4 + m) * 64 + l];
#pragma unroll
    for (int i = 0; i < 8; ++i) { u32x4 af = tfrag(raw, i);
#pragma unroll
      for (int m = 0; m < 4; ++m) acc[i][m] = mfma(af, bf[m], acc[i][m]); }
  }
  f32x4* pp = (f32x4*)a.part + ((size_t)((br * 16 + b) * 8 + hg) * 4 + w) * 32 * 64 + l;
#pragma unroll
  for (int m = 0; m < 4; ++m)
#pragma unroll
    for (int rho = 0; rho < 4; ++rho)
#pragma unroll
      for (int hf = 0; hf < 2; ++hf)
        pp[((m * 4 + rho) * 2 + hf) * 64] = f32x4{acc[4*hf][m][rho], acc[4*hf+1][m][rho], acc[4*hf+2][m][rho], acc[4*hf+3][m][rho]};
}
__global__ __launch_bounds__(256) void k_red(Args a) {
  int g = blockIdx.x * 256 + threadIdx.x;
  int l = g & 63, mr = (g >> 6) & 15, w = (g >> 10) & 3, bb = g >> 12;
  int r = l & 15, q = l >> 4, m = mr >> 2, rho = mr & 3;
  f32x4 s0 = {0,0,0,0}, s1 = {0,0,0,0};
#pragma unroll
  for (int hg = 0; hg < 8; ++hg) { const f32x4* pp = (const f32x4*)a.part + ((size_t)(bb * 8 + hg) * 4 + w) * 32 * 64 + l;
    s0 += pp[(mr * 2) * 64]; s1 += pp[(mr * 2 + 1) * 64]; }
  u32x4 o = {cvtpk(s0[0], s0[1]), cvtpk(s0[2], s0[3]), cvtpk(s1[0], s1[1]), cvtpk(s1[2], s1[3])};
  int br = bb >> 4, b = bb & 15;
  *(u32x4*)(a.out + ((size_t)(b * 64 + 16 * m + r) * 1024 + br * 512 + w * 128 + 32 * rho + 8 * q)) = o;
}
extern "C" void kernel_launch(void* const* d_in, const int* in_sizes, int n_in, void* d_out, int out_size, void* d_ws, size_t ws_size, hipStream_t stream) {
  Args a;
  for (int br = 0; br < 2; ++br) {
    a.x[br] = (const uint16_t*)d_in[5*br+0]; a.k[br] = (const uint16_t*)d_in[5*br+1]; a.v[br] = (const uint16_t*)d_in[5*br+2];
    a.wq[br] = (const uint16_t*)d_in[5*br+3]; a.wo[br] = (const uint16_t*)d_in[5*br+4]; }
  a.part = (float*)d_ws; a.out = (uint16_t*)d_out;
  k_main<<<256, 256, 0, stream>>>(a);
  k_red<<<512, 256, 0, stream>>>(a);
}
```

```cpp
#include <hip/hip_runtime.h>
#include <stdint.h>
struct Br { const char* x; const char* k; const char* v; const char* wq; const char* wo; char* out; char* ws; char* ws2; };
struct __attribute__((aligned(64))) Args { Br br[2]; };

__global__ __launch_bounds__(256, 1) void k_fused(Args a) {
  __shared__ uint4 smem[10240];
  asm volatile(
    "s_bfe_u32 s77, %1, 0x10002\n"
    "s_lshl_b32 s75, s77, 6\n"
    "s_load_dwordx16 s[4:19], %0, s75\n"
    "v_readfirstlane_b32 s60, v0\n"
    "s_lshr_b32 s62, %1, 4\n"
    "v_and_b32 v0, 63, v0\n"
    "s_lshl_b32 s61, %1, 1\n"
    "s_and_b32 s61, s61, 6\n"
    "s_bfe_u32 s75, %1, 0x10003\n"
    "s_or_b32 s61, s61, s75\n"
    "s_lshr_b32 s60, s60, 6\n"
    "s_lshl_b32 s25, s62, 16\n"
    "s_mov_b32 s86, 0x10000\n"
    "s_mov_b32 s87, 0x20000\n"
    "s_and_b32 s63, s62, 7\n"
    "v_lshrrev_b32 v27, 3, v0\n"
    "v_and_b32 v28, 7, v0\n"
    "v_and_b32 v40, 6, v27\n"
    "v_xor_b32 v40, v40, v28\n"
    "v_lshlrev_b32 v40, 4, v40\n"
    "v_lshl_add_u32 v40, v27, 10, v40\n"
    "s_lshl_b32 s74, s60, 14\n"
    "v_add_u32 v78, s74, v40\n"
    "v_add_u32 v79, 7168, v78\n"
    "s_lshl_b32 s90, s60, 11\n"
    "s_lshl_b32 s22, s61, 2\n"
    "s_add_u32 s22, s22, s60\n"
    "s_lshl_b32 s23, s62, 5\n"
    "s_add_u32 s23, s23, s22\n"
    "s_lshl_b32 s22, s22, 8\n"
    "s_lshr_b32 s65, s62, 3\n"
    "s_mul_i32 s66, s60, 24576\n"
    "s_add_u32 s66, s66, 65536\n"
    "s_mov_b32 s38, 0x3fe100\n"
    "s_mov_b32 s39, 0x20000\n"
    "s_lshl_b32 s64, s63, 19\n"
    "s_lshl_b32 s74, s65, 7\n"
    "s_add_u32 s64, s64, s74\n"
    "v_lshrrev_b32 v35, 1, v28\n"
    "v_bfe_u32 v36, v27, 1, 1\n"
    "v_xor_b32 v35, v35, v36\n"
    "v_and_b32 v36, 1, v28\n"
    "v_lshlrev_b32 v35, 5, v35\n"
    "v_lshl_add_u32 v37, v36, 4, v35\n"
    "v_xor_b32 v38, 64, v37\n"
    "v_lshl_add_u32 v8, v27, 13, v37\n"
    "v_lshl_add_u32 v9, v27, 13, v38\n"
    "v_add_u32 v10, 129024, v8\n"
    "v_add_u32 v11, 193536, v9\n"
    "v_add_u32 v9, 64512, v9\n"
    "s_and_b32 s74, s61, 1\n"
    "s_lshl_b32 s74, s74, 1\n"
    "s_xor_b32 s76, s74, 0\n"
    "s_lshl_b32 s75, s76, 13\n"
    "s_add_u32 s68, s75, s90\n"
    "s_add_u32 s76, s76, s63\n"
    "s_and_b32 s76, s76, 7\n"
    "s_lshl_b32 s92, s76, 7\n"
    "s_xor_b32 s76, s74, 1\n"
    "s_lshl_b32 s75, s76, 13\n"
    "s_add_u32 s69, s75, s90\n"
    "s_add_u32 s76, s76, s63\n"
    "s_and_b32 s76, s76, 7\n"
    "s_lshl_b32 s93, s76, 7\n"
    "s_xor_b32 s76, s74, 2\n"
    "s_lshl_b32 s75, s76, 13\n"
    "s_add_u32 s70, s75, s90\n"
    "s_add_u32 s76, s76, s63\n"
    "s_and_b32 s76, s76, 7\n"
    "s_lshl_b32 s94, s76, 7\n"
    "s_xor_b32 s76, s74, 3\n"
    "s_lshl_b32 s75, s76, 13\n"
    "s_add_u32 s71, s75, s90\n"
    "s_add_u32 s76, s76, s63\n"
    "s_and_b32 s76, s76, 7\n"
    "s_lshl_b32 s95, s76, 7\n"
    "s_waitcnt lgkmcnt(0)\n"
    "s_add_u32 s84, s4, s25\n"
    "s_addc_u32 s85, s5, 0\n"
    "s_mov_b32 m0, s68\n"
    "s_nop 0\n"
    "buffer_load_dwordx4 v78, s[84:87], s92 offen lds\n"
    "buffer_load_dwordx4 v79, s[84:87], s92 offen offset:1024 lds\n"
    "s_mov_b32 m0, s69\n"
    "s_nop 0\n"
    "buffer_load_dwordx4 v78, s[84:87], s93 offen lds\n"
    "buffer_load_dwordx4 v79, s[84:87], s93 offen offset:1024 lds\n"
    "s_mov_b32 m0, s70\n"
    "s_nop 0\n"
    "buffer_load_dwordx4 v78, s[84:87], s94 offen lds\n"
    "buffer_load_dwordx4 v79, s[84:87], s94 offen offset:1024 lds\n"
    "s_mov_b32 m0, s71\n"
    "s_nop 0\n"
    "buffer_load_dwordx4 v78, s[84:87], s95 offen lds\n"
    "buffer_load_dwordx4 v79, s[84:87], s95 offen offset:1024 lds\n"
    "s_add_u32 s36, s10, s22\n"
    "s_addc_u32 s37, s11, 0\n"
    "s_add_u32 m0, s66, 0\n"
    "s_nop 0\n"
    "buffer_load_dwordx4 v8, s[36:39], s64 offen offset:0 lds\n"
    "buffer_load_dwordx4 v9, s[36:39], s64 offen offset:1024 lds\n"
    "buffer_load_dwordx4 v10, s[36:39], s64 offen offset:2048 lds\n"
    "buffer_load_dwordx4 v11, s[36:39], s64 offen offset:3072 lds\n"
    "s_getpc_b64 s[96:97]\n"
    "L_pc_%=:\n"
    "s_lshr_b32 s74, %1, 3\n"
    "s_lshl_b32 s74, s74, 2\n"
    "s_add_u32 s74, s74, s60\n"
    "s_lshl_b32 s74, s74, 8\n"
    "v_and_b32 v42, 1, v0\n"
    "v_lshl_add_u32 v42, v42, 7, s74\n"
    "v_min_u32 v42, L_fin_%=-L_pc_%=-4, v42\n"
    "global_load_dword v42, v42, s[96:97]\n"
    "s_add_u32 s88, s64, 262144\n"
    "s_xor_b32 s89, s64, 128\n"
    "s_add_u32 m0, s66, 4096\n"
    "s_nop 0\n"
    "buffer_load_dwordx4 v8, s[36:39], s89 offen offset:0 lds\n"
    "buffer_load_dwordx4 v9, s[36:39], s89 offen offset:1024 lds\n"
    "buffer_load_dwordx4 v10, s[36:39], s89 offen offset:2048 lds\n"
    "buffer_load_dwordx4 v11, s[36:39], s89 offen offset:3072 lds\n"
    "v_lshl_add_u32 v64, v27, 8, v37\n"
    "v_lshl_add_u32 v65, v27, 8, v38\n"
    "s_lshl_b32 s23, s23, 14\n"
    "s_lshl_b32 s20, s77, 4\n"
    "s_add_u32 s20, s20, s62\n"
    "s_mul_i32 s20, s20, 0x90000\n"
    "s_lshl_b32 s21, s62, 17\n"
    "s_lshl_b32 s24, s61, 19\n"
    "v_lshrrev_b32 v41, 4, v0\n"
    "v_and_b32 v34, 15, v0\n"
    "s_and_b32 s26, s60, 1\n"
    "s_lshl_b32 s26, s26, 13\n"
    "s_add_u32 s40, s6, s23\n"
    "s_addc_u32 s41, s7, 0\n"
    "s_mov_b32 s42, 0x4000\n"
    "s_mov_b32 s43, 0x20000\n"
    "v_xor_b32 v35, v34, v41\n"
    "v_lshlrev_b32 v35, 4, v35\n"
    "v_lshl_add_u32 v66, v41, 8, v35\n"
    "v_xor_b32 v67, 64, v66\n"
    "v_xor_b32 v68, 128, v66\n"
    "v_xor_b32 v69, 192, v66\n"
    "s_mov_b32 s67, -2\n"
    "s_and_b32 s74, s61, 0\n"
    "s_xor_b32 s74, s74, s62\n"
    "s_lshl_b32 s74, s74, 1\n"
    "s_or_b32 s76, s65, s74\n"
    "s_and_b32 s76, s76, 3\n"
    "s_lshl_b32 s76, s76, 2\n"
    "s_lshr_b32 s68, 17185, s76\n"
    "s_and_b32 s68, s68, 15\n"
    "s_add_u32 s68, s68, -1\n"
    "s_lshr_b32 s69, 34661, s76\n"
    "s_and_b32 s69, s69, 15\n"
    "s_add_u32 s69, s69, -1\n"
    "s_lshr_b32 s70, 52137, s76\n"
    "s_and_b32 s70, s70, 15\n"
    "s_add_u32 s70, s70, -1\n"
    "v_lshrrev_b32 v35, 3, v34\n"
    "v_and_b32 v36, 7, v34\n"
    "v_lshlrev_b32 v36, 7, v36\n"
    "v_lshl_add_u32 v35, v35, 10, v36\n"
    "v_and_b32 v36, 6, v34\n"
    "v_xor_b32 v36, v36, v41\n"
    "v_lshl_add_u32 v2, v36, 4, v35\n"
    "v_xor_b32 v3, 64, v2\n"
    "v_bfe_u32 v35, v0, 2, 2\n"
    "v_and_b32 v36, 3, v0\n"
    "v_and_b32 v37, 1, v41\n"
    "v_lshrrev_b32 v39, 1, v35\n"
    "v_lshl_add_u32 v37, v37, 1, v39\n"
    "v_lshl_add_u32 v38, v41, 3, v35\n"
    "v_lshlrev_b32 v38, 7, v38\n"
    "v_lshl_add_u32 v38, v36, 3, v38\n"
    "v_lshl_add_u32 v38, v37, 5, v38\n"
    "v_add_u32 v4, s66, v38\n"
    "v_xor_b32 v5, 32, v4\n"
    "v_xor_b32 v6, 64, v4\n"
    "v_xor_b32 v7, 96, v4\n"
    "s_add_u32 m0, s66, 8192\n"
    "s_nop 0\n"
    "buffer_load_dwordx4 v8, s[36:39], s88 offen offset:0 lds\n"
    "buffer_load_dwordx4 v9, s[36:39], s88 offen offset:1024 lds\n"
    "buffer_load_dwordx4 v10, s[36:39], s88 offen offset:2048 lds\n"
    "buffer_load_dwordx4 v11, s[36:39], s88 offen offset:3072 lds\n"
    "s_xor_b32 s64, s88, 128\n"
    "s_add_u32 s52, s16, s20\n"
    "s_addc_u32 s53, s17, 0\n"
    "s_mov_b32 s54, 0x90000\n"
    "s_mov_b32 s55, 0x20000\n"
    "s_lshl_b32 s11, s61, 2\n"
    "s_add_u32 s11, s11, s60\n"
    "v_sub_u32 v43, s61, v0\n"
    "v_add_u32 v43, -1, v43\n"
    "v_and_b32 v43, 7, v43\n"
    "v_lshlrev_b32 v43, 7, v43\n"
    "s_lshl_b32 s19, s11, 10\n"
    "s_add_u32 s19, s19, 0x80000\n"
    "v_add_u32 v43, s19, v43\n"
    "s_lshl_b32 s19, s11, 7\n"
    "s_add_u32 s19, s19, 0x88000\n"
    "v_mov_b32 v44, s19\n"
    "v_cmp_gt_u32 vcc, 7, v0\n"
    "s_nop 1\n"
    "v_cndmask_b32 v21, v44, v43, vcc\n"
    "buffer_load_dword v23, v21, s[52:55], 0 offen sc1\n"
    "v_add_u32 v43, s61, v0\n"
    "v_add_u32 v43, 1, v43\n"
    "v_and_b32 v43, 7, v43\n"
    "v_lshlrev_b32 v43, 2, v43\n"
    "v_add_u32 v43, s60, v43\n"
    "v_lshlrev_b32 v45, 7, v43\n"
    "v_add_u32 v45, 0x88000, v45\n"
    "v_mov_b32 v46, 0x7ff00000\n"
    "v_cndmask_b32 v22, v46, v45, vcc\n"
    "v_lshlrev_b32 v43, 10, v43\n"
    "s_lshl_b32 s19, s61, 7\n"
    "s_add_u32 s19, s19, 0x80000\n"
    "v_add_u32 v43, s19, v43\n"
    "v_cndmask_b32 v25, v46, v43, vcc\n"
    "v_cmp_eq_u32 vcc, 63, v0\n"
    "s_nop 1\n"
    "v_cndmask_b32 v26, v46, v44, vcc\n"
    "s_add_u32 s44, s8, s23\n"
    "s_addc_u32 s45, s9, 0\n"
    "s_mov_b32 s46, 0x4000\n"
    "s_mov_b32 s47, 0x20000\n"
    "s_add_u32 s48, s12, s24\n"
    "s_addc_u32 s49, s13, 0\n"
    "s_mov_b32 s50, 0x80000\n"
    "s_mov_b32 s51, 0x20000\n"
    "s_add_u32 s56, s14, s21\n"
    "s_addc_u32 s57, s15, 0\n"
    "s_mov_b32 s58, 0x1fc00\n"
    "s_mov_b32 s59, 0x20000\n"
    "s_mov_b32 s78, 0x3e0293ee\n"
    "s_and_b32 s27, s63, 7\n"
    "s_add_u32 s27, s27, s61\n"
    "s_and_b32 s27, s27, 7\n"
    "s_lshl_b32 s28, s65, 18\n"
    "s_lshl_b32 s79, s61, 13\n"
    "s_add_u32 s79, s79, s90\n"
    "s_lshl_b32 s81, s61, 16\n"
    "s_add_u32 s81, s81, s90\n"
    "s_bfe_u32 s25, s62, 131072\n"
    "s_mov_b32 s83, 0\n"
    "s_mov_b32 s82, 0\n"
    "s_mov_b32 s29, 127\n"
    "s_lshl_b32 s77, s60, 13\n"
    "v_lshlrev_b32 v1, 4, v0\n"
    "s_lshl_b32 s11, s60, 14\n"
    "v_add_u32 v20, s11, v1\n"
    "s_lshl_b32 s11, s65, 15\n"
    "v_add_u32 v29, s11, v1\n"
    "v_xor_b32 v30, 0x8000, v29\n"
    "v_lshl_add_u32 v38, v41, 2, v35\n"
    "v_bfe_u32 v39, v38, 1, 2\n"
    "v_xor_b32 v39, s60, v39\n"
    "v_lshlrev_b32 v38, 7, v38\n"
    "v_lshl_add_u32 v38, v39, 5, v38\n"
    "v_lshl_add_u32 v16, v36, 3, v38\n"
    "v_add_u32 v17, 65536, v16\n"
    "v_add_u32 v18, 114688, v16\n"
    "v_lshrrev_b32 v35, 1, v28\n"
    "v_lshrrev_b32 v36, 1, v27\n"
    "v_xor_b32 v35, v35, v36\n"
    "v_and_b32 v36, 1, v28\n"
    "v_lshlrev_b32 v35, 5, v35\n"
    "v_lshl_add_u32 v35, v36, 4, v35\n"
    "v_lshl_add_u32 v35, v27, 10, v35\n"
    "s_lshl_b32 s11, s60, 16\n"
    "v_add_u32 v12, s11, v35\n"
    "v_add_u32 v13, 7168, v12\n"
    "v_add_u32 v14, 14336, v12\n"
    "v_add_u32 v15, 21504, v12\n"
    "v_lshrrev_b32 v35, 2, v34\n"
    "v_and_b32 v36, 3, v34\n"
    "v_lshl_add_u32 v35, v35, 3, v36\n"
    "v_lshrrev_b32 v36, 1, v41\n"
    "v_xor_b32 v36, v36, v34\n"
    "v_lshlrev_b32 v36, 4, v36\n"
    "v_lshl_add_u32 v35, v35, 8, v36\n"
    "v_and_b32 v36, 1, v41\n"
    "v_lshl_add_u32 v35, v36, 3, v35\n"
    "s_lshl_b32 s11, s65, 7\n"
    "v_xor_b32 v35, s11, v35\n"
    "s_add_u32 s11, s66, 16384\n"
    "v_add_u32 v70, s11, v35\n"
    "v_lshlrev_b32 v35, 4, v28\n"
    "v_lshl_add_u32 v19, v27, 11, v35\n"
    "s_lshl_b32 s11, s27, 7\n"
    "s_lshl_b32 s19, s60, 15\n"
    "s_add_u32 s20, s11, s19\n"
    "s_add_u32 s21, s20, 16384\n"
    "v_xor_b32 v71, 32, v70\n"
    "v_xor_b32 v72, 64, v70\n"
    "v_xor_b32 v73, 96, v70\n"
    "v_xor_b32 v74, 128, v70\n"
    "v_xor_b32 v75, 160, v70\n"
    "v_xor_b32 v76, 192, v70\n"
    "v_xor_b32 v77, 224, v70\n"
    "v_mov_b32 v35, 0x3f803f80\n"
    "v_accvgpr_write_b32 a80, v35\n"
    "v_accvgpr_write_b32 a81, v35\n"
    "v_accvgpr_write_b32 a82, v35\n"
    "v_accvgpr_write_b32 a83, v35\n"
    "s_waitcnt vmcnt(14)\n"
    "s_barrier\n"
    "ds_read_b128 a[16:19], v2 offset:0\n"
    "ds_read_b128 a[20:23], v2 offset:2048\n"
    "ds_read_b128 a[24:27], v2 offset:4096\n"
    "ds_read_b128 a[28:31], v2 offset:6144\n"
    "s_waitcnt vmcnt(10)\n"
    "ds_read_b64_tr_b16 a[0:1], v4 offset:0\n"
    "ds_read_b64_tr_b16 a[2:3], v4 offset:512\n"
    "ds_read_b64_tr_b16 a[4:5], v5 offset:0\n"
    "ds_read_b64_tr_b16 a[6:7], v5 offset:512\n"
    "s_cmp_eq_u32 s68, 0\n"
    "s_cbranch_scc1 L_o0_%=\n"
    "L_b0_%=:\n"
    "s_waitcnt lgkmcnt(2)\n"
    "v_mfma_f32_16x16x32_bf16 v[128:131], a[0:3], a[16:19], 0\n"
    "s_add_u32 m0, s66, 12288\n"
    "ds_read_b64_tr_b16 a[8:9], v6 offset:0\n"
    "v_mfma_f32_16x16x32_bf16 v[160:163], a[0:3], a[20:23], 0\n"
    "ds_read_b64_tr_b16 a[10:11], v6 offset:512\n"
    "v_mfma_f32_16x16x32_bf16 v[192:195], a[0:3], a[24:27], 0\n"
    "buffer_load_dwordx4 v8, s[36:39], s64 offen offset:0 lds\n"
    "v_mfma_f32_16x16x32_bf16 v[224:227], a[0:3], a[28:31], 0\n"
    "s_waitcnt lgkmcnt(2)\n"
    "v_mfma_f32_16x16x32_bf16 v[132:135], a[4:7], a[16:19], 0\n"
    "ds_read_b64_tr_b16 a[0:1], v7 offset:0\n"
    "v_mfma_f32_16x16x32_bf16 v[164:167], a[4:7], a[20:23], 0\n"
    "ds_read_b64_tr_b16 a[2:3], v7 offset:512\n"
    "v_mfma_f32_16x16x32_bf16 v[196:199], a[4:7], a[24:27], 0\n"
    "buffer_load_dwordx4 v9, s[36:39], s64 offen offset:1024 lds\n"
    "v_mfma_f32_16x16x32_bf16 v[228:231], a[4:7], a[28:31], 0\n"
    "s_waitcnt lgkmcnt(2)\n"
    "ds_read_b128 a[32:35], v3 offset:0\n"
    "v_mfma_f32_16x16x32_bf16 v[136:139], a[8:11], a[16:19], 0\n"
    "s_cmp_eq_u32 s67, 0\n"
    "s_cbranch_scc1 L_w0_%=\n"
    "s_waitcnt vmcnt(7)\n"
    "L_w0_%=:\n"
    "s_waitcnt vmcnt(23)\n"
    "ds_read_b64_tr_b16 a[4:5], v4 offset:4096\n"
    "v_mfma_f32_16x16x32_bf16 v[168:171], a[8:11], a[20:23], 0\n"
    "ds_read_b64_tr_b16 a[6:7], v4 offset:4608\n"
    "v_mfma_f32_16x16x32_bf16 v[200:203], a[8:11], a[24:27], 0\n"
    "buffer_load_dwordx4 v10, s[36:39], s64 offen offset:2048 lds\n"
    "v_mfma_f32_16x16x32_bf16 v[232:235], a[8:11], a[28:31], 0\n"
    "s_waitcnt lgkmcnt(3)\n"
    "v_mfma_f32_16x16x32_bf16 v[140:143], a[0:3], a[16:19], 0\n"
    "ds_read_b64_tr_b16 a[8:9], v5 offset:4096\n"
    "v_mfma_f32_16x16x32_bf16 v[172:175], a[0:3], a[20:23], 0\n"
    "ds_read_b64_tr_b16 a[10:11], v5 offset:4608\n"
    "v_mfma_f32_16x16x32_bf16 v[204:207], a[0:3], a[24:27], 0\n"
    "buffer_load_dwordx4 v11, s[36:39], s64 offen offset:3072 lds\n"
    "v_mfma_f32_16x16x32_bf16 v[236:239], a[0:3], a[28:31], 0\n"
    "s_waitcnt lgkmcnt(2)\n"
    "ds_read_b128 a[36:39], v3 offset:2048\n"
    "v_mfma_f32_16x16x32_bf16 v[144:147], a[4:7], a[16:19], 0\n"
    "s_xor_b32 s64, s64, 128\n"
    "s_add_u32 s64, s64, 262144\n"
    "s_and_b32 s64, s64, 0x3fffff\n"
    "s_add_u32 m0, s66, 0\n"
    "ds_read_b64_tr_b16 a[0:1], v6 offset:4096\n"
    "v_mfma_f32_16x16x32_bf16 v[176:179], a[4:7], a[20:23], 0\n"
    "ds_read_b64_tr_b16 a[2:3], v6 offset:4608\n"
    "v_mfma_f32_16x16x32_bf16 v[208:211], a[4:7], a[24:27], 0\n"
    "buffer_load_dwordx4 v8, s[36:39], s64 offen offset:0 lds\n"
    "v_mfma_f32_16x16x32_bf16 v[240:243], a[4:7], a[28:31], 0\n"
    "s_waitcnt lgkmcnt(3)\n"
    "ds_read_b128 a[40:43], v3 offset:4096\n"
    "v_mfma_f32_16x16x32_bf16 v[148:151], a[8:11], a[16:19], 0\n"
    "ds_read_b64_tr_b16 a[4:5], v7 offset:4096\n"
    "v_mfma_f32_16x16x32_bf16 v[180:183], a[8:11], a[20:23], 0\n"
    "ds_read_b64_tr_b16 a[6:7], v7 offset:4608\n"
    "v_mfma_f32_16x16x32_bf16 v[212:215], a[8:11], a[24:27], 0\n"
    "buffer_load_dwordx4 v9, s[36:39], s64 offen offset:1024 lds\n"
    "v_mfma_f32_16x16x32_bf16 v[244:247], a[8:11], a[28:31], 0\n"
    "s_waitcnt lgkmcnt(3)\n"
    "ds_read_b128 a[44:47], v3 offset:6144\n"
    "v_mfma_f32_16x16x32_bf16 v[152:155], a[0:3], a[16:19], 0\n"
    "s_cmp_eq_u32 s67, 0\n"
    "s_cbranch_scc1 L_w1_%=\n"
    "s_waitcnt vmcnt(7)\n"
    "L_w1_%=:\n"
    "s_waitcnt vmcnt(23)\n"
    "ds_read_b64_tr_b16 a[8:9], v4 offset:8192\n"
    "v_mfma_f32_16x16x32_bf16 v[184:187], a[0:3], a[20:23], 0\n"
    "ds_read_b64_tr_b16 a[10:11], v4 offset:8704\n"
    "v_mfma_f32_16x16x32_bf16 v[216:219], a[0:3], a[24:27], 0\n"
    "buffer_load_dwordx4 v10, s[36:39], s64 offen offset:2048 lds\n"
    "v_mfma_f32_16x16x32_bf16 v[248:251], a[0:3], a[28:31], 0\n"
    "s_waitcnt lgkmcnt(3)\n"
    "v_mfma_f32_16x16x32_bf16 v[156:159], a[4:7], a[16:19], 0\n"
    "ds_read_b64_tr_b16 a[0:1], v5 offset:8192\n"
    "v_mfma_f32_16x16x32_bf16 v[188:191], a[4:7], a[20:23], 0\n"
    "ds_read_b64_tr_b16 a[2:3], v5 offset:8704\n"
    "v_mfma_f32_16x16x32_bf16 v[220:223], a[4:7], a[24:27], 0\n"
    "buffer_load_dwordx4 v11, s[36:39], s64 offen offset:3072 lds\n"
    "v_mfma_f32_16x16x32_bf16 v[252:255], a[4:7], a[28:31], 0\n"
    "s_xor_b32 s64, s64, 128\n"
    "s_cmp_eq_u32 s68, 1\n"
    "s_cbranch_scc1 L_o1_%=\n"
    "L_b1_%=:\n"
    "s_waitcnt lgkmcnt(2)\n"
    "v_mfma_f32_16x16x32_bf16 v[128:131], a[8:11], a[32:35], v[128:131]\n"
    "s_add_u32 m0, s66, 4096\n"
    "ds_read_b64_tr_b16 a[4:5], v6 offset:8192\n"
    "v_mfma_f32_16x16x32_bf16 v[160:163], a[8:11], a[36:39], v[160:163]\n"
    "ds_read_b64_tr_b16 a[6:7], v6 offset:8704\n"
    "v_mfma_f32_16x16x32_bf16 v[192:195], a[8:11], a[40:43], v[192:195]\n"
    "buffer_load_dwordx4 v8, s[36:39], s64 offen offset:0 lds\n"
    "v_mfma_f32_16x16x32_bf16 v[224:227], a[8:11], a[44:47], v[224:227]\n"
    "s_waitcnt lgkmcnt(2)\n"
    "v_mfma_f32_16x16x32_bf16 v[132:135], a[0:3], a[32:35], v[132:135]\n"
    "ds_read_b64_tr_b16 a[8:9], v7 offset:8192\n"
    "v_mfma_f32_16x16x32_bf16 v[164:167], a[0:3], a[36:39], v[164:167]\n"
    "ds_read_b64_tr_b16 a[10:11], v7 offset:8704\n"
    "v_mfma_f32_16x16x32_bf16 v[196:199], a[0:3], a[40:43], v[196:199]\n"
    "buffer_load_dwordx4 v9, s[36:39], s64 offen offset:1024 lds\n"
    "v_mfma_f32_16x16x32_bf16 v[228:231], a[0:3], a[44:47], v[228:231]\n"
    "s_waitcnt lgkmcnt(2)\n"
    "ds_read_b128 a[16:19], v2 offset:8192\n"
    "v_mfma_f32_16x16x32_bf16 v[136:139], a[4:7], a[32:35], v[136:139]\n"
    "s_cmp_eq_u32 s67, 1\n"
    "s_cbranch_scc1 L_w2_%=\n"
    "s_waitcnt vmcnt(6)\n"
    "L_w2_%=:\n"
    "s_waitcnt vmcnt(22)\n"
    "ds_read_b64_tr_b16 a[0:1], v4 offset:12288\n"
    "v_mfma_f32_16x16x32_bf16 v[168:171], a[4:7], a[36:39], v[168:171]\n"
    "ds_read_b64_tr_b16 a[2:3], v4 offset:12800\n"
    "v_mfma_f32_16x16x32_bf16 v[200:203], a[4:7], a[40:43], v[200:203]\n"
    "buffer_load_dwordx4 v10, s[36:39], s64 offen offset:2048 lds\n"
    "v_mfma_f32_16x16x32_bf16 v[232:235], a[4:7], a[44:47], v[232:235]\n"
    "s_waitcnt lgkmcnt(3)\n"
    "v_mfma_f32_16x16x32_bf16 v[140:143], a[8:11], a[32:35], v[140:143]\n"
    "ds_read_b64_tr_b16 a[4:5], v5 offset:12288\n"
    "v_mfma_f32_16x16x32_bf16 v[172:175], a[8:11], a[36:39], v[172:175]\n"
    "ds_read_b64_tr_b16 a[6:7], v5 offset:12800\n"
    "v_mfma_f32_16x16x32_bf16 v[204:207], a[8:11], a[40:43], v[204:207]\n"
    "buffer_load_dwordx4 v11, s[36:39], s64 offen offset:3072 lds\n"
    "v_mfma_f32_16x16x32_bf16 v[236:239], a[8:11], a[44:47], v[236:239]\n"
    "s_waitcnt lgkmcnt(2)\n"
    "ds_read_b128 a[20:23], v2 offset:10240\n"
    "v_mfma_f32_16x16x32_bf16 v[144:147], a[0:3], a[32:35], v[144:147]\n"
    "s_xor_b32 s64, s64, 128\n"
    "s_add_u32 s64, s64, 262144\n"
    "s_and_b32 s64, s64, 0x3fffff\n"
    "s_add_u32 m0, s66, 8192\n"
    "ds_read_b64_tr_b16 a[8:9], v6 offset:12288\n"
    "v_mfma_f32_16x16x32_bf16 v[176:179], a[0:3], a[36:39], v[176:179]\n"
    "ds_read_b64_tr_b16 a[10:11], v6 offset:12800\n"
    "v_mfma_f32_16x16x32_bf16 v[208:211], a[0:3], a[40:43], v[208:211]\n"
    "buffer_load_dwordx4 v8, s[36:39], s64 offen offset:0 lds\n"
    "v_mfma_f32_16x16x32_bf16 v[240:243], a[0:3], a[44:47], v[240:243]\n"
    "s_waitcnt lgkmcnt(3)\n"
    "ds_read_b128 a[24:27], v2 offset:12288\n"
    "v_mfma_f32_16x16x32_bf16 v[148:151], a[4:7], a[32:35], v[148:151]\n"
    "ds_read_b64_tr_b16 a[0:1], v7 offset:12288\n"
    "v_mfma_f32_16x16x32_bf16 v[180:183], a[4:7], a[36:39], v[180:183]\n"
    "ds_read_b64_tr_b16 a[2:3], v7 offset:12800\n"
    "v_mfma_f32_16x16x32_bf16 v[212:215], a[4:7], a[40:43], v[212:215]\n"
    "buffer_load_dwordx4 v9, s[36:39], s64 offen offset:1024 lds\n"
    "v_mfma_f32_16x16x32_bf16 v[244:247], a[4:7], a[44:47], v[244:247]\n"
    "s_waitcnt lgkmcnt(3)\n"
    "ds_read_b128 a[28:31], v2 offset:14336\n"
    "v_mfma_f32_16x16x32_bf16 v[152:155], a[8:11], a[32:35], v[152:155]\n"
    "s_cmp_eq_u32 s67, 1\n"
    "s_cbranch_scc1 L_w3_%=\n"
    "s_waitcnt vmcnt(6)\n"
    "L_w3_%=:\n"
    "s_waitcnt vmcnt(22)\n"
    "ds_read_b64_tr_b16 a[4:5], v4 offset:0\n"
    "v_mfma_f32_16x16x32_bf16 v[184:187], a[8:11], a[36:39], v[184:187]\n"
    "ds_read_b64_tr_b16 a[6:7], v4 offset:512\n"
    "v_mfma_f32_16x16x32_bf16 v[216:219], a[8:11], a[40:43], v[216:219]\n"
    "buffer_load_dwordx4 v10, s[36:39], s64 offen offset:2048 lds\n"
    "v_mfma_f32_16x16x32_bf16 v[248:251], a[8:11], a[44:47], v[248:251]\n"
    "s_waitcnt lgkmcnt(3)\n"
    "v_mfma_f32_16x16x32_bf16 v[156:159], a[0:3], a[32:35], v[156:159]\n"
    "ds_read_b64_tr_b16 a[8:9], v5 offset:0\n"
    "v_mfma_f32_16x16x32_bf16 v[188:191], a[0:3], a[36:39], v[188:191]\n"
    "ds_read_b64_tr_b16 a[10:11], v5 offset:512\n"
    "v_mfma_f32_16x16x32_bf16 v[220:223], a[0:3], a[40:43], v[220:223]\n"
    "buffer_load_dwordx4 v11, s[36:39], s64 offen offset:3072 lds\n"
    "v_mfma_f32_16x16x32_bf16 v[252:255], a[0:3], a[44:47], v[252:255]\n"
    "s_xor_b32 s64, s64, 128\n"
    "s_cmp_eq_u32 s68, 2\n"
    "s_cbranch_scc1 L_o2_%=\n"
    "L_b2_%=:\n"
    "s_waitcnt lgkmcnt(2)\n"
    "v_mfma_f32_16x16x32_bf16 v[128:131], a[4:7], a[16:19], v[128:131]\n"
    "s_add_u32 m0, s66, 12288\n"
    "ds_read_b64_tr_b16 a[0:1], v6 offset:0\n"
    "v_mfma_f32_16x16x32_bf16 v[160:163], a[4:7], a[20:23], v[160:163]\n"
    "ds_read_b64_tr_b16 a[2:3], v6 offset:512\n"
    "v_mfma_f32_16x16x32_bf16 v[192:195], a[4:7], a[24:27], v[192:195]\n"
    "buffer_load_dwordx4 v8, s[36:39], s64 offen offset:0 lds\n"
    "v_mfma_f32_16x16x32_bf16 v[224:227], a[4:7], a[28:31], v[224:227]\n"
    "s_waitcnt lgkmcnt(2)\n"
    "v_mfma_f32_16x16x32_bf16 v[132:135], a[8:11], a[16:19], v[132:135]\n"
    "ds_read_b64_tr_b16 a[4:5], v7 offset:0\n"
    "v_mfma_f32_16x16x32_bf16 v[164:167], a[8:11], a[20:23], v[164:167]\n"
    "ds_read_b64_tr_b16 a[6:7], v7 offset:512\n"
    "v_mfma_f32_16x16x32_bf16 v[196:199], a[8:11], a[24:27], v[196:199]\n"
    "buffer_load_dwordx4 v9, s[36:39], s64 offen offset:1024 lds\n"
    "v_mfma_f32_16x16x32_bf16 v[228:231], a[8:11], a[28:31], v[228:231]\n"
    "s_waitcnt lgkmcnt(2)\n"
    "ds_read_b128 a[32:35], v3 offset:8192\n"
    "v_mfma_f32_16x16x32_bf16 v[136:139], a[0:3], a[16:19], v[136:139]\n"
    "s_cmp_eq_u32 s67, 2\n"
    "s_cbranch_scc1 L_w4_%=\n"
    "s_waitcnt vmcnt(6)\n"
    "L_w4_%=:\n"
    "s_waitcnt vmcnt(22)\n"
    "ds_read_b64_tr_b16 a[8:9], v4 offset:4096\n"
    "v_mfma_f32_16x16x32_bf16 v[168:171], a[0:3], a[20:23], v[168:171]\n"
    "ds_read_b64_tr_b16 a[10:11], v4 offset:4608\n"
    "v_mfma_f32_16x16x32_bf16 v[200:203], a[0:3], a[24:27], v[200:203]\n"
    "buffer_load_dwordx4 v10, s[36:39], s64 offen offset:2048 lds\n"
    "v_mfma_f32_16x16x32_bf16 v[232:235], a[0:3], a[28:31], v[232:235]\n"
    "s_waitcnt lgkmcnt(3)\n"
    "v_mfma_f32_16x16x32_bf16 v[140:143], a[4:7], a[16:19], v[140:143]\n"
    "ds_read_b64_tr_b16 a[0:1], v5 offset:4096\n"
    "v_mfma_f32_16x16x32_bf16 v[172:175], a[4:7], a[20:23], v[172:175]\n"
    "ds_read_b64_tr_b16 a[2:3], v5 offset:4608\n"
    "v_mfma_f32_16x16x32_bf16 v[204:207], a[4:7], a[24:27], v[204:207]\n"
    "buffer_load_dwordx4 v11, s[36:39], s64 offen offset:3072 lds\n"
    "v_mfma_f32_16x16x32_bf16 v[236:239], a[4:7], a[28:31], v[236:239]\n"
    "s_waitcnt lgkmcnt(2)\n"
    "ds_read_b128 a[36:39], v3 offset:10240\n"
    "v_mfma_f32_16x16x32_bf16 v[144:147], a[8:11], a[16:19], v[144:147]\n"
    "s_xor_b32 s64, s64, 128\n"
    "s_add_u32 s64, s64, 262144\n"
    "s_and_b32 s64, s64, 0x3fffff\n"
    "s_add_u32 m0, s66, 0\n"
    "ds_read_b64_tr_b16 a[4:5], v6 offset:4096\n"
    "v_mfma_f32_16x16x32_bf16 v[176:179], a[8:11], a[20:23], v[176:179]\n"
    "ds_read_b64_tr_b16 a[6:7], v6 offset:4608\n"
    "v_mfma_f32_16x16x32_bf16 v[208:211], a[8:11], a[24:27], v[208:211]\n"
    "buffer_load_dwordx4 v8, s[36:39], s64 offen offset:0 lds\n"
    "v_mfma_f32_16x16x32_bf16 v[240:243], a[8:11], a[28:31], v[240:243]\n"
    "s_waitcnt lgkmcnt(3)\n"
    "ds_read_b128 a[40:43], v3 offset:12288\n"
    "v_mfma_f32_16x16x32_bf16 v[148:151], a[0:3], a[16:19], v[148:151]\n"
    "ds_read_b64_tr_b16 a[8:9], v7 offset:4096\n"
    "v_mfma_f32_16x16x32_bf16 v[180:183], a[0:3], a[20:23], v[180:183]\n"
    "ds_read_b64_tr_b16 a[10:11], v7 offset:4608\n"
    "v_mfma_f32_16x16x32_bf16 v[212:215], a[0:3], a[24:27], v[212:215]\n"
    "buffer_load_dwordx4 v9, s[36:39], s64 offen offset:1024 lds\n"
    "v_mfma_f32_16x16x32_bf16 v[244:247], a[0:3], a[28:31], v[244:247]\n"
    "s_waitcnt lgkmcnt(3)\n"
    "ds_read_b128 a[44:47], v3 offset:14336\n"
    "v_mfma_f32_16x16x32_bf16 v[152:155], a[4:7], a[16:19], v[152:155]\n"
    "s_cmp_eq_u32 s67, 2\n"
    "s_cbranch_scc1 L_w5_%=\n"
    "s_waitcnt vmcnt(6)\n"
    "L_w5_%=:\n"
    "s_waitcnt vmcnt(22)\n"
    "ds_read_b64_tr_b16 a[0:1], v4 offset:8192\n"
    "v_mfma_f32_16x16x32_bf16 v[184:187], a[4:7], a[20:23], v[184:187]\n"
    "ds_read_b64_tr_b16 a[2:3], v4 offset:8704\n"
    "v_mfma_f32_16x16x32_bf16 v[216:219], a[4:7], a[24:27], v[216:219]\n"
    "buffer_load_dwordx4 v10, s[36:39], s64 offen offset:2048 lds\n"
    "v_mfma_f32_16x16x32_bf16 v[248:251], a[4:7], a[28:31], v[248:251]\n"
    "s_waitcnt lgkmcnt(3)\n"
    "v_mfma_f32_16x16x32_bf16 v[156:159], a[8:11], a[16:19], v[156:159]\n"
    "ds_read_b64_tr_b16 a[4:5], v5 offset:8192\n"
    "v_mfma_f32_16x16x32_bf16 v[188:191], a[8:11], a[20:23], v[188:191]\n"
    "ds_read_b64_tr_b16 a[6:7], v5 offset:8704\n"
    "v_mfma_f32_16x16x32_bf16 v[220:223], a[8:11], a[24:27], v[220:223]\n"
    "buffer_load_dwordx4 v11, s[36:39], s64 offen offset:3072 lds\n"
    "v_mfma_f32_16x16x32_bf16 v[252:255], a[8:11], a[28:31], v[252:255]\n"
    "s_xor_b32 s64, s64, 128\n"
    "s_cmp_eq_u32 s68, 3\n"
    "s_cbranch_scc1 L_o3_%=\n"
    "L_b3_%=:\n"
    "s_waitcnt lgkmcnt(2)\n"
    "v_mfma_f32_16x16x32_bf16 v[128:131], a[0:3], a[32:35], v[128:131]\n"
    "s_add_u32 m0, s66, 4096\n"
    "ds_read_b64_tr_b16 a[8:9], v6 offset:8192\n"
    "v_mfma_f32_16x16x32_bf16 v[160:163], a[0:3], a[36:39], v[160:163]\n"
    "ds_read_b64_tr_b16 a[10:11], v6 offset:8704\n"
    "v_mfma_f32_16x16x32_bf16 v[192:195], a[0:3], a[40:43], v[192:195]\n"
    "buffer_load_dwordx4 v8, s[36:39], s64 offen offset:0 lds\n"
    "v_mfma_f32_16x16x32_bf16 v[224:227], a[0:3], a[44:47], v[224:227]\n"
    "s_waitcnt lgkmcnt(2)\n"
    "v_mfma_f32_16x16x32_bf16 v[132:135], a[4:7], a[32:35], v[132:135]\n"
    "ds_read_b64_tr_b16 a[0:1], v7 offset:8192\n"
    "v_mfma_f32_16x16x32_bf16 v[164:167], a[4:7], a[36:39], v[164:167]\n"
    "ds_read_b64_tr_b16 a[2:3], v7 offset:8704\n"
    "v_mfma_f32_16x16x32_bf16 v[196:199], a[4:7], a[40:43], v[196:199]\n"
    "buffer_load_dwordx4 v9, s[36:39], s64 offen offset:1024 lds\n"
    "v_mfma_f32_16x16x32_bf16 v[228:231], a[4:7], a[44:47], v[228:231]\n"
    "s_waitcnt lgkmcnt(2)\n"
    "ds_read_b128 a[16:19], v2 offset:16384\n"
    "v_mfma_f32_16x16x32_bf16 v[136:139], a[8:11], a[32:35], v[136:139]\n"
    "s_cmp_eq_u32 s67, 3\n"
    "s_cbranch_scc1 L_w6_%=\n"
    "s_waitcnt vmcnt(6)\n"
    "L_w6_%=:\n"
    "s_waitcnt vmcnt(22)\n"
    "ds_read_b64_tr_b16 a[4:5], v4 offset:12288\n"
    "v_mfma_f32_16x16x32_bf16 v[168:171], a[8:11], a[36:39], v[168:171]\n"
    "ds_read_b64_tr_b16 a[6:7], v4 offset:12800\n"
    "v_mfma_f32_16x16x32_bf16 v[200:203], a[8:11], a[40:43], v[200:203]\n"
    "buffer_load_dwordx4 v10, s[36:39], s64 offen offset:2048 lds\n"
    "v_mfma_f32_16x16x32_bf16 v[232:235], a[8:11], a[44:47], v[232:235]\n"
    "s_waitcnt lgkmcnt(3)\n"
    "v_mfma_f32_16x16x32_bf16 v[140:143], a[0:3], a[32:35], v[140:143]\n"
    "ds_read_b64_tr_b16 a[8:9], v5 offset:12288\n"
    "v_mfma_f32_16x16x32_bf16 v[172:175], a[0:3], a[36:39], v[172:175]\n"
    "ds_read_b64_tr_b16 a[10:11], v5 offset:12800\n"
    "v_mfma_f32_16x16x32_bf16 v[204:207], a[0:3], a[40:43], v[204:207]\n"
    "buffer_load_dwordx4 v11, s[36:39], s64 offen offset:3072 lds\n"
    "v_mfma_f32_16x16x32_bf16 v[236:239], a[0:3], a[44:47], v[236:239]\n"
    "s_waitcnt lgkmcnt(2)\n"
    "ds_read_b128 a[20:23], v2 offset:18432\n"
    "v_mfma_f32_16x16x32_bf16 v[144:147], a[4:7], a[32:35], v[144:147]\n"
    "s_xor_b32 s64, s64, 128\n"
    "s_add_u32 s64, s64, 262144\n"
    "s_and_b32 s64, s64, 0x3fffff\n"
    "s_add_u32 m0, s66, 8192\n"
    "ds_read_b64_tr_b16 a[0:1], v6 offset:12288\n"
    "v_mfma_f32_16x16x32_bf16 v[176:179], a[4:7], a[36:39], v[176:179]\n"
    "ds_read_b64_tr_b16 a[2:3], v6 offset:12800\n"
    "v_mfma_f32_16x16x32_bf16 v[208:211], a[4:7], a[40:43], v[208:211]\n"
    "buffer_load_dwordx4 v8, s[36:39], s64 offen offset:0 lds\n"
    "v_mfma_f32_16x16x32_bf16 v[240:243], a[4:7], a[44:47], v[240:243]\n"
    "s_waitcnt lgkmcnt(3)\n"
    "ds_read_b128 a[24:27], v2 offset:20480\n"
    "v_mfma_f32_16x16x32_bf16 v[148:151], a[8:11], a[32:35], v[148:151]\n"
    "ds_read_b64_tr_b16 a[4:5], v7 offset:12288\n"
    "v_mfma_f32_16x16x32_bf16 v[180:183], a[8:11], a[36:39], v[180:183]\n"
    "ds_read_b64_tr_b16 a[6:7], v7 offset:12800\n"
    "v_mfma_f32_16x16x32_bf16 v[212:215], a[8:11], a[40:43], v[212:215]\n"
    "buffer_load_dwordx4 v9, s[36:39], s64 offen offset:1024 lds\n"
    "v_mfma_f32_16x16x32_bf16 v[244:247], a[8:11], a[44:47], v[244:247]\n"
    "s_waitcnt lgkmcnt(3)\n"
    "ds_read_b128 a[28:31], v2 offset:22528\n"
    "v_mfma_f32_16x16x32_bf16 v[152:155], a[0:3], a[32:35], v[152:155]\n"
    "s_cmp_eq_u32 s67, 3\n"
    "s_cbranch_scc1 L_w7_%=\n"
    "s_waitcnt vmcnt(6)\n"
    "L_w7_%=:\n"
    "s_waitcnt vmcnt(22)\n"
    "ds_read_b64_tr_b16 a[8:9], v4 offset:0\n"
    "v_mfma_f32_16x16x32_bf16 v[184:187], a[0:3], a[36:39], v[184:187]\n"
    "ds_read_b64_tr_b16 a[10:11], v4 offset:512\n"
    "v_mfma_f32_16x16x32_bf16 v[216:219], a[0:3], a[40:43], v[216:219]\n"
    "buffer_load_dwordx4 v10, s[36:39], s64 offen offset:2048 lds\n"
    "v_mfma_f32_16x16x32_bf16 v[248:251], a[0:3], a[44:47], v[248:251]\n"
    "s_waitcnt lgkmcnt(3)\n"
    "v_mfma_f32_16x16x32_bf16 v[156:159], a[4:7], a[32:35], v[156:159]\n"
    "ds_read_b64_tr_b16 a[0:1], v5 offset:0\n"
    "v_mfma_f32_16x16x32_bf16 v[188:191], a[4:7], a[36:39], v[188:191]\n"
    "ds_read_b64_tr_b16 a[2:3], v5 offset:512\n"
    "v_mfma_f32_16x16x32_bf16 v[220:223], a[4:7], a[40:43], v[220:223]\n"
    "buffer_load_dwordx4 v11, s[36:39], s64 offen offset:3072 lds\n"
    "v_mfma_f32_16x16x32_bf16 v[252:255], a[4:7], a[44:47], v[252:255]\n"
    "s_xor_b32 s64, s64, 128\n"
    "s_cmp_eq_u32 s69, 4\n"
    "s_cbranch_scc1 L_o4_%=\n"
    "L_b4_%=:\n"
    "s_waitcnt lgkmcnt(2)\n"
    "v_mfma_f32_16x16x32_bf16 v[128:131], a[8:11], a[16:19], v[128:131]\n"
    "s_add_u32 m0, s66, 12288\n"
    "ds_read_b64_tr_b16 a[4:5], v6 offset:0\n"
    "v_mfma_f32_16x16x32_bf16 v[160:163], a[8:11], a[20:23], v[160:163]\n"
    "ds_read_b64_tr_b16 a[6:7], v6 offset:512\n"
    "v_mfma_f32_16x16x32_bf16 v[192:195], a[8:11], a[24:27], v[192:195]\n"
    "buffer_load_dwordx4 v8, s[36:39], s64 offen offset:0 lds\n"
    "v_mfma_f32_16x16x32_bf16 v[224:227], a[8:11], a[28:31], v[224:227]\n"
    "s_waitcnt lgkmcnt(2)\n"
    "v_mfma_f32_16x16x32_bf16 v[132:135], a[0:3], a[16:19], v[132:135]\n"
    "ds_read_b64_tr_b16 a[8:9], v7 offset:0\n"
    "v_mfma_f32_16x16x32_bf16 v[164:167], a[0:3], a[20:23], v[164:167]\n"
    "ds_read_b64_tr_b16 a[10:11], v7 offset:512\n"
    "v_mfma_f32_16x16x32_bf16 v[196:199], a[0:3], a[24:27], v[196:199]\n"
    "buffer_load_dwordx4 v9, s[36:39], s64 offen offset:1024 lds\n"
    "v_mfma_f32_16x16x32_bf16 v[228:231], a[0:3], a[28:31], v[228:231]\n"
    "s_waitcnt lgkmcnt(2)\n"
    "ds_read_b128 a[32:35], v3 offset:16384\n"
    "v_mfma_f32_16x16x32_bf16 v[136:139], a[4:7], a[16:19], v[136:139]\n"
    "s_cmp_eq_u32 s67, 4\n"
    "s_cbranch_scc1 L_w8_%=\n"
    "s_waitcnt vmcnt(6)\n"
    "L_w8_%=:\n"
    "s_waitcnt vmcnt(14)\n"
    "ds_read_b64_tr_b16 a[0:1], v4 offset:4096\n"
    "v_mfma_f32_16x16x32_bf16 v[168:171], a[4:7], a[20:23], v[168:171]\n"
    "ds_read_b64_tr_b16 a[2:3], v4 offset:4608\n"
    "v_mfma_f32_16x16x32_bf16 v[200:203], a[4:7], a[24:27], v[200:203]\n"
    "buffer_load_dwordx4 v10, s[36:39], s64 offen offset:2048 lds\n"
    "v_mfma_f32_16x16x32_bf16 v[232:235], a[4:7], a[28:31], v[232:235]\n"
    "s_waitcnt lgkmcnt(3)\n"
    "v_mfma_f32_16x16x32_bf16 v[140:143], a[8:11], a[16:19], v[140:143]\n"
    "ds_read_b64_tr_b16 a[4:5], v5 offset:4096\n"
    "v_mfma_f32_16x16x32_bf16 v[172:175], a[8:11], a[20:23], v[172:175]\n"
    "ds_read_b64_tr_b16 a[6:7], v5 offset:4608\n"
    "v_mfma_f32_16x16x32_bf16 v[204:207], a[8:11], a[24:27], v[204:207]\n"
    "buffer_load_dwordx4 v11, s[36:39], s64 offen offset:3072 lds\n"
    "v_mfma_f32_16x16x32_bf16 v[236:239], a[8:11], a[28:31], v[236:239]\n"
    "s_waitcnt lgkmcnt(2)\n"
    "ds_read_b128 a[36:39], v3 offset:18432\n"
    "v_mfma_f32_16x16x32_bf16 v[144:147], a[0:3], a[16:19], v[144:147]\n"
    "s_xor_b32 s64, s64, 128\n"
    "s_add_u32 s64, s64, 262144\n"
    "s_and_b32 s64, s64, 0x3fffff\n"
    "s_add_u32 m0, s66, 0\n"
    "ds_read_b64_tr_b16 a[8:9], v6 offset:4096\n"
    "v_mfma_f32_16x16x32_bf16 v[176:179], a[0:3], a[20:23], v[176:179]\n"
    "ds_read_b64_tr_b16 a[10:11], v6 offset:4608\n"
    "v_mfma_f32_16x16x32_bf16 v[208:211], a[0:3], a[24:27], v[208:211]\n"
    "buffer_load_dwordx4 v8, s[36:39], s64 offen offset:0 lds\n"
    "v_mfma_f32_16x16x32_bf16 v[240:243], a[0:3], a[28:31], v[240:243]\n"
    "s_waitcnt lgkmcnt(3)\n"
    "ds_read_b128 a[40:43], v3 offset:20480\n"
    "v_mfma_f32_16x16x32_bf16 v[148:151], a[4:7], a[16:19], v[148:151]\n"
    "ds_read_b64_tr_b16 a[0:1], v7 offset:4096\n"
    "v_mfma_f32_16x16x32_bf16 v[180:183], a[4:7], a[20:23], v[180:183]\n"
    "ds_read_b64_tr_b16 a[2:3], v7 offset:4608\n"
    "v_mfma_f32_16x16x32_bf16 v[212:215], a[4:7], a[24:27], v[212:215]\n"
    "buffer_load_dwordx4 v9, s[36:39], s64 offen offset:1024 lds\n"
    "v_mfma_f32_16x16x32_bf16 v[244:247], a[4:7], a[28:31], v[244:247]\n"
    "s_waitcnt lgkmcnt(3)\n"
    "ds_read_b128 a[44:47], v3 offset:22528\n"
    "v_mfma_f32_16x16x32_bf16 v[152:155], a[8:11], a[16:19], v[152:155]\n"
    "s_cmp_eq_u32 s67, 4\n"
    "s_cbranch_scc1 L_w9_%=\n"
    "s_waitcnt vmcnt(6)\n"
    "L_w9_%=:\n"
    "s_waitcnt vmcnt(14)\n"
    "ds_read_b64_tr_b16 a[4:5], v4 offset:8192\n"
    "v_mfma_f32_16x16x32_bf16 v[184:187], a[8:11], a[20:23], v[184:187]\n"
    "ds_read_b64_tr_b16 a[6:7], v4 offset:8704\n"
    "v_mfma_f32_16x16x32_bf16 v[216:219], a[8:11], a[24:27], v[216:219]\n"
    "buffer_load_dwordx4 v10, s[36:39], s64 offen offset:2048 lds\n"
    "v_mfma_f32_16x16x32_bf16 v[248:251], a[8:11], a[28:31], v[248:251]\n"
    "s_waitcnt lgkmcnt(3)\n"
    "v_mfma_f32_16x16x32_bf16 v[156:159], a[0:3], a[16:19], v[156:159]\n"
    "ds_read_b64_tr_b16 a[8:9], v5 offset:8192\n"
    "v_mfma_f32_16x16x32_bf16 v[188:191], a[0:3], a[20:23], v[188:191]\n"
    "ds_read_b64_tr_b16 a[10:11], v5 offset:8704\n"
    "v_mfma_f32_16x16x32_bf16 v[220:223], a[0:3], a[24:27], v[220:223]\n"
    "buffer_load_dwordx4 v11, s[36:39], s64 offen offset:3072 lds\n"
    "v_mfma_f32_16x16x32_bf16 v[252:255], a[0:3], a[28:31], v[252:255]\n"
    "s_xor_b32 s64, s64, 128\n"
    "s_cmp_eq_u32 s69, 5\n"
    "s_cbranch_scc1 L_o5_%=\n"
    "L_b5_%=:\n"
    "s_waitcnt lgkmcnt(2)\n"
    "v_mfma_f32_16x16x32_bf16 v[128:131], a[4:7], a[32:35], v[128:131]\n"
    "s_add_u32 m0, s66, 4096\n"
    "ds_read_b64_tr_b16 a[0:1], v6 offset:8192\n"
    "v_mfma_f32_16x16x32_bf16 v[160:163], a[4:7], a[36:39], v[160:163]\n"
    "ds_read_b64_tr_b16 a[2:3], v6 offset:8704\n"
    "v_mfma_f32_16x16x32_bf16 v[192:195], a[4:7], a[40:43], v[192:195]\n"
    "buffer_load_dwordx4 v8, s[36:39], s64 offen offset:0 lds\n"
    "v_mfma_f32_16x16x32_bf16 v[224:227], a[4:7], a[44:47], v[224:227]\n"
    "s_waitcnt lgkmcnt(2)\n"
    "v_mfma_f32_16x16x32_bf16 v[132:135], a[8:11], a[32:35], v[132:135]\n"
    "ds_read_b64_tr_b16 a[4:5], v7 offset:8192\n"
    "v_mfma_f32_16x16x32_bf16 v[164:167], a[8:11], a[36:39], v[164:167]\n"
    "ds_read_b64_tr_b16 a[6:7], v7 offset:8704\n"
    "v_mfma_f32_16x16x32_bf16 v[196:199], a[8:11], a[40:43], v[196:199]\n"
    "buffer_load_dwordx4 v9, s[36:39], s64 offen offset:1024 lds\n"
    "v_mfma_f32_16x16x32_bf16 v[228:231], a[8:11], a[44:47], v[228:231]\n"
    "s_waitcnt lgkmcnt(2)\n"
    "ds_read_b128 a[16:19], v2 offset:24576\n"
    "v_mfma_f32_16x16x32_bf16 v[136:139], a[0:3], a[32:35], v[136:139]\n"
    "s_cmp_eq_u32 s67, 5\n"
    "s_cbranch_scc1 L_w10_%=\n"
    "s_waitcnt vmcnt(6)\n"
    "L_w10_%=:\n"
    "s_waitcnt vmcnt(14)\n"
    "ds_read_b64_tr_b16 a[8:9], v4 offset:12288\n"
    "v_mfma_f32_16x16x32_bf16 v[168:171], a[0:3], a[36:39], v[168:171]\n"
    "ds_read_b64_tr_b16 a[10:11], v4 offset:12800\n"
    "v_mfma_f32_16x16x32_bf16 v[200:203], a[0:3], a[40:43], v[200:203]\n"
    "buffer_load_dwordx4 v10, s[36:39], s64 offen offset:2048 lds\n"
    "v_mfma_f32_16x16x32_bf16 v[232:235], a[0:3], a[44:47], v[232:235]\n"
    "s_waitcnt lgkmcnt(3)\n"
    "v_mfma_f32_16x16x32_bf16 v[140:143], a[4:7], a[32:35], v[140:143]\n"
    "ds_read_b64_tr_b16 a[0:1], v5 offset:12288\n"
    "v_mfma_f32_16x16x32_bf16 v[172:175], a[4:7], a[36:39], v[172:175]\n"
    "ds_read_b64_tr_b16 a[2:3], v5 offset:12800\n"
    "v_mfma_f32_16x16x32_bf16 v[204:207], a[4:7], a[40:43], v[204:207]\n"
    "buffer_load_dwordx4 v11, s[36:39], s64 offen offset:3072 lds\n"
    "v_mfma_f32_16x16x32_bf16 v[236:239], a[4:7], a[44:47], v[236:239]\n"
    "s_waitcnt lgkmcnt(2)\n"
    "ds_read_b128 a[20:23], v2 offset:26624\n"
    "v_mfma_f32_16x16x32_bf16 v[144:147], a[8:11], a[32:35], v[144:147]\n"
    "s_xor_b32 s64, s64, 128\n"
    "s_add_u32 s64, s64, 262144\n"
    "s_and_b32 s64, s64, 0x3fffff\n"
    "s_add_u32 m0, s66, 8192\n"
    "ds_read_b64_tr_b16 a[4:5], v6 offset:12288\n"
    "v_mfma_f32_16x16x32_bf16 v[176:179], a[8:11], a[36:39], v[176:179]\n"
    "ds_read_b64_tr_b16 a[6:7], v6 offset:12800\n"
    "v_mfma_f32_16x16x32_bf16 v[208:211], a[8:11], a[40:43], v[208:211]\n"
    "buffer_load_dwordx4 v8, s[36:39], s64 offen offset:0 lds\n"
    "v_mfma_f32_16x16x32_bf16 v[240:243], a[8:11], a[44:47], v[240:243]\n"
    "s_waitcnt lgkmcnt(3)\n"
    "ds_read_b128 a[24:27], v2 offset:28672\n"
    "v_mfma_f32_16x16x32_bf16 v[148:151], a[0:3], a[32:35], v[148:151]\n"
    "ds_read_b64_tr_b16 a[8:9], v7 offset:12288\n"
    "v_mfma_f32_16x16x32_bf16 v[180:183], a[0:3], a[36:39], v[180:183]\n"
    "ds_read_b64_tr_b16 a[10:11], v7 offset:12800\n"
    "v_mfma_f32_16x16x32_bf16 v[212:215], a[0:3], a[40:43], v[212:215]\n"
    "buffer_load_dwordx4 v9, s[36:39], s64 offen offset:1024 lds\n"
    "v_mfma_f32_16x16x32_bf16 v[244:247], a[0:3], a[44:47], v[244:247]\n"
    "s_waitcnt lgkmcnt(3)\n"
    "ds_read_b128 a[28:31], v2 offset:30720\n"
    "v_mfma_f32_16x16x32_bf16 v[152:155], a[4:7], a[32:35], v[152:155]\n"
    "s_cmp_eq_u32 s67, 5\n"
    "s_cbranch_scc1 L_w11_%=\n"
    "s_waitcnt vmcnt(6)\n"
    "L_w11_%=:\n"
    "s_waitcnt vmcnt(14)\n"
    "ds_read_b64_tr_b16 a[0:1], v4 offset:0\n"
    "v_mfma_f32_16x16x32_bf16 v[184:187], a[4:7], a[36:39], v[184:187]\n"
    "ds_read_b64_tr_b16 a[2:3], v4 offset:512\n"
    "v_mfma_f32_16x16x32_bf16 v[216:219], a[4:7], a[40:43], v[216:219]\n"
    "buffer_load_dwordx4 v10, s[36:39], s64 offen offset:2048 lds\n"
    "v_mfma_f32_16x16x32_bf16 v[248:251], a[4:7], a[44:47], v[248:251]\n"
    "s_waitcnt lgkmcnt(3)\n"
    "v_mfma_f32_16x16x32_bf16 v[156:159], a[8:11], a[32:35], v[156:159]\n"
    "ds_read_b64_tr_b16 a[4:5], v5 offset:0\n"
    "v_mfma_f32_16x16x32_bf16 v[188:191], a[8:11], a[36:39], v[188:191]\n"
    "ds_read_b64_tr_b16 a[6:7], v5 offset:512\n"
    "v_mfma_f32_16x16x32_bf16 v[220:223], a[8:11], a[40:43], v[220:223]\n"
    "buffer_load_dwordx4 v11, s[36:39], s64 offen offset:3072 lds\n"
    "v_mfma_f32_16x16x32_bf16 v[252:255], a[8:11], a[44:47], v[252:255]\n"
    "s_xor_b32 s64, s64, 128\n"
    "s_cmp_eq_u32 s69, 6\n"
    "s_cbranch_scc1 L_o6_%=\n"
    "L_b6_%=:\n"
    "s_waitcnt lgkmcnt(2)\n"
    "v_mfma_f32_16x16x32_bf16 v[128:131], a[0:3], a[16:19], v[128:131]\n"
    "s_add_u32 m0, s66, 12288\n"
    "ds_read_b64_tr_b16 a[8:9], v6 offset:0\n"
    "v_mfma_f32_16x16x32_bf16 v[160:163], a[0:3], a[20:23], v[160:163]\n"
    "s_sub_u32 s4, s61, 1\n"
    "s_and_b32 s4, s4, 7\n"
    "ds_read_b64_tr_b16 a[10:11], v6 offset:512\n"
    "v_mfma_f32_16x16x32_bf16 v[192:195], a[0:3], a[24:27], v[192:195]\n"
    "buffer_load_dwordx4 v8, s[36:39], s64 offen offset:0 lds\n"
    "v_mfma_f32_16x16x32_bf16 v[224:227], a[0:3], a[28:31], v[224:227]\n"
    "s_waitcnt lgkmcnt(2)\n"
    "s_lshl_b32 s4, s4, 13\n"
    "s_add_u32 s4, s4, s81\n"
    "v_mfma_f32_16x16x32_bf16 v[132:135], a[4:7], a[16:19], v[132:135]\n"
    "ds_read_b64_tr_b16 a[0:1], v7 offset:0\n"
    "v_mfma_f32_16x16x32_bf16 v[164:167], a[4:7], a[20:23], v[164:167]\n"
    "s_add_u32 s12, s61, 1\n"
    "s_and_b32 s12, s12, 7\n"
    "ds_read_b64_tr_b16 a[2:3], v7 offset:512\n"
    "v_mfma_f32_16x16x32_bf16 v[196:199], a[4:7], a[24:27], v[196:199]\n"
    "buffer_load_dwordx4 v9, s[36:39], s64 offen offset:1024 lds\n"
    "v_mfma_f32_16x16x32_bf16 v[228:231], a[4:7], a[28:31], v[228:231]\n"
    "s_waitcnt lgkmcnt(2)\n"
    "s_lshl_b32 s12, s12, 16\n"
    "s_add_u32 s12, s12, s79\n"
    "ds_read_b128 a[32:35], v3 offset:24576\n"
    "v_mfma_f32_16x16x32_bf16 v[136:139], a[8:11], a[16:19], v[136:139]\n"
    "s_cmp_eq_u32 s67, 6\n"
    "s_cbranch_scc1 L_w12_%=\n"
    "s_waitcnt vmcnt(6)\n"
    "L_w12_%=:\n"
    "s_waitcnt vmcnt(14)\n"
    "ds_read_b64_tr_b16 a[4:5], v4 offset:4096\n"
    "v_mfma_f32_16x16x32_bf16 v[168:171], a[8:11], a[20:23], v[168:171]\n"
    "s_sub_u32 s5, s61, 2\n"
    "s_and_b32 s5, s5, 7\n"
    "ds_read_b64_tr_b16 a[6:7], v4 offset:4608\n"
    "v_mfma_f32_16x16x32_bf16 v[200:203], a[8:11], a[24:27], v[200:203]\n"
    "buffer_load_dwordx4 v10, s[36:39], s64 offen offset:2048 lds\n"
    "v_mfma_f32_16x16x32_bf16 v[232:235], a[8:11], a[28:31], v[232:235]\n"
    "s_waitcnt lgkmcnt(3)\n"
    "s_lshl_b32 s5, s5, 13\n"
    "s_add_u32 s5, s5, s81\n"
    "v_mfma_f32_16x16x32_bf16 v[140:143], a[0:3], a[16:19], v[140:143]\n"
    "ds_read_b64_tr_b16 a[8:9], v5 offset:4096\n"
    "v_mfma_f32_16x16x32_bf16 v[172:175], a[0:3], a[20:23], v[172:175]\n"
    "s_add_u32 s13, s61, 2\n"
    "s_and_b32 s13, s13, 7\n"
    "ds_read_b64_tr_b16 a[10:11], v5 offset:4608\n"
    "v_mfma_f32_16x16x32_bf16 v[204:207], a[0:3], a[24:27], v[204:207]\n"
    "buffer_load_dwordx4 v11, s[36:39], s64 offen offset:3072 lds\n"
    "v_mfma_f32_16x16x32_bf16 v[236:239], a[0:3], a[28:31], v[236:239]\n"
    "s_waitcnt lgkmcnt(2)\n"
    "s_lshl_b32 s13, s13, 16\n"
    "s_add_u32 s13, s13, s79\n"
    "ds_read_b128 a[36:39], v3 offset:26624\n"
    "v_mfma_f32_16x16x32_bf16 v[144:147], a[4:7], a[16:19], v[144:147]\n"
    "s_xor_b32 s64, s64, 128\n"
    "s_add_u32 s64, s64, 262144\n"
    "s_and_b32 s64, s64, 0x3fffff\n"
    "s_add_u32 m0, s66, 0\n"
    "ds_read_b64_tr_b16 a[0:1], v6 offset:4096\n"
    "v_mfma_f32_16x16x32_bf16 v[176:179], a[4:7], a[20:23], v[176:179]\n"
    "s_sub_u32 s6, s61, 3\n"
    "s_and_b32 s6, s6, 7\n"
    "ds_read_b64_tr_b16 a[2:3], v6 offset:4608\n"
    "v_mfma_f32_16x16x32_bf16 v[208:211], a[4:7], a[24:27], v[208:211]\n"
    "buffer_load_dwordx4 v8, s[36:39], s64 offen offset:0 lds\n"
    "v_mfma_f32_16x16x32_bf16 v[240:243], a[4:7], a[28:31], v[240:243]\n"
    "s_waitcnt lgkmcnt(3)\n"
    "s_lshl_b32 s6, s6, 13\n"
    "s_add_u32 s6, s6, s81\n"
    "ds_read_b128 a[40:43], v3 offset:28672\n"
    "v_mfma_f32_16x16x32_bf16 v[148:151], a[8:11], a[16:19], v[148:151]\n"
    "ds_read_b64_tr_b16 a[4:5], v7 offset:4096\n"
    "v_mfma_f32_16x16x32_bf16 v[180:183], a[8:11], a[20:23], v[180:183]\n"
    "s_add_u32 s14, s61, 3\n"
    "s_and_b32 s14, s14, 7\n"
    "ds_read_b64_tr_b16 a[6:7], v7 offset:4608\n"
    "v_mfma_f32_16x16x32_bf16 v[212:215], a[8:11], a[24:27], v[212:215]\n"
    "buffer_load_dwordx4 v9, s[36:39], s64 offen offset:1024 lds\n"
    "v_mfma_f32_16x16x32_bf16 v[244:247], a[8:11], a[28:31], v[244:247]\n"
    "s_waitcnt lgkmcnt(3)\n"
    "s_lshl_b32 s14, s14, 16\n"
    "s_add_u32 s14, s14, s79\n"
    "ds_read_b128 a[44:47], v3 offset:30720\n"
    "v_mfma_f32_16x16x32_bf16 v[152:155], a[0:3], a[16:19], v[152:155]\n"
    "s_cmp_eq_u32 s67, 6\n"
    "s_cbranch_scc1 L_w13_%=\n"
    "s_waitcnt vmcnt(6)\n"
    "L_w13_%=:\n"
    "s_waitcnt vmcnt(14)\n"
    "ds_read_b64_tr_b16 a[8:9], v4 offset:8192\n"
    "v_mfma_f32_16x16x32_bf16 v[184:187], a[0:3], a[20:23], v[184:187]\n"
    "s_sub_u32 s7, s61, 4\n"
    "s_and_b32 s7, s7, 7\n"
    "ds_read_b64_tr_b16 a[10:11], v4 offset:8704\n"
    "v_mfma_f32_16x16x32_bf16 v[216:219], a[0:3], a[24:27], v[216:219]\n"
    "buffer_load_dwordx4 v10, s[36:39], s64 offen offset:2048 lds\n"
    "v_mfma_f32_16x16x32_bf16 v[248:251], a[0:3], a[28:31], v[248:251]\n"
    "s_waitcnt lgkmcnt(3)\n"
    "s_lshl_b32 s7, s7, 13\n"
    "s_add_u32 s7, s7, s81\n"
    "v_mfma_f32_16x16x32_bf16 v[156:159], a[4:7], a[16:19], v[156:159]\n"
    "ds_read_b64_tr_b16 a[0:1], v5 offset:8192\n"
    "v_mfma_f32_16x16x32_bf16 v[188:191], a[4:7], a[20:23], v[188:191]\n"
    "s_add_u32 s15, s61, 4\n"
    "s_and_b32 s15, s15, 7\n"
    "ds_read_b64_tr_b16 a[2:3], v5 offset:8704\n"
    "v_mfma_f32_16x16x32_bf16 v[220:223], a[4:7], a[24:27], v[220:223]\n"
    "buffer_load_dwordx4 v11, s[36:39], s64 offen offset:3072 lds\n"
    "v_mfma_f32_16x16x32_bf16 v[252:255], a[4:7], a[28:31], v[252:255]\n"
    "s_lshl_b32 s15, s15, 16\n"
    "s_add_u32 s15, s15, s79\n"
    "s_xor_b32 s64, s64, 128\n"
    "s_barrier\n"
    "s_cmp_eq_u32 s69, 7\n"
    "s_cbranch_scc1 L_o7_%=\n"
    "L_b7_%=:\n"
    "s_waitcnt lgkmcnt(2)\n"
    "v_mfma_f32_16x16x32_bf16 v[128:131], a[8:11], a[32:35], v[128:131]\n"
    "s_add_u32 m0, s66, 4096\n"
    "ds_read_b64_tr_b16 a[4:5], v6 offset:8192\n"
    "v_mfma_f32_16x16x32_bf16 v[160:163], a[8:11], a[36:39], v[160:163]\n"
    "s_sub_u32 s8, s61, 5\n"
    "s_and_b32 s8, s8, 7\n"
    "ds_read_b64_tr_b16 a[6:7], v6 offset:8704\n"
    "v_mfma_f32_16x16x32_bf16 v[192:195], a[8:11], a[40:43], v[192:195]\n"
    "buffer_load_dwordx4 v8, s[36:39], s64 offen offset:0 lds\n"
    "v_mfma_f32_16x16x32_bf16 v[224:227], a[8:11], a[44:47], v[224:227]\n"
    "s_waitcnt lgkmcnt(2)\n"
    "s_lshl_b32 s8, s8, 13\n"
    "s_add_u32 s8, s8, s81\n"
    "v_mfma_f32_16x16x32_bf16 v[132:135], a[0:3], a[32:35], v[132:135]\n"
    "ds_read_b64_tr_b16 a[8:9], v7 offset:8192\n"
    "v_mfma_f32_16x16x32_bf16 v[164:167], a[0:3], a[36:39], v[164:167]\n"
    "s_add_u32 s16, s61, 5\n"
    "s_and_b32 s16, s16, 7\n"
    "ds_read_b64_tr_b16 a[10:11], v7 offset:8704\n"
    "v_mfma_f32_16x16x32_bf16 v[196:199], a[0:3], a[40:43], v[196:199]\n"
    "buffer_load_dwordx4 v9, s[36:39], s64 offen offset:1024 lds\n"
    "v_mfma_f32_16x16x32_bf16 v[228:231], a[0:3], a[44:47], v[228:231]\n"
    "s_waitcnt lgkmcnt(2)\n"
    "s_lshl_b32 s16, s16, 16\n"
    "s_add_u32 s16, s16, s79\n"
    "ds_read_b128 a[16:19], v2 offset:32768\n"
    "v_mfma_f32_16x16x32_bf16 v[136:139], a[4:7], a[32:35], v[136:139]\n"
    "s_cmp_eq_u32 s67, 7\n"
    "s_cbranch_scc1 L_w14_%=\n"
    "s_waitcnt vmcnt(6)\n"
    "L_w14_%=:\n"
    "s_waitcnt vmcnt(14)\n"
    "ds_read_b64_tr_b16 a[0:1], v4 offset:12288\n"
    "v_mfma_f32_16x16x32_bf16 v[168:171], a[4:7], a[36:39], v[168:171]\n"
    "s_sub_u32 s9, s61, 6\n"
    "s_and_b32 s9, s9, 7\n"
    "ds_read_b64_tr_b16 a[2:3], v4 offset:12800\n"
    "v_mfma_f32_16x16x32_bf16 v[200:203], a[4:7], a[40:43], v[200:203]\n"
    "buffer_load_dwordx4 v10, s[36:39], s64 offen offset:2048 lds\n"
    "v_mfma_f32_16x16x32_bf16 v[232:235], a[4:7], a[44:47], v[232:235]\n"
    "s_waitcnt lgkmcnt(3)\n"
    "s_lshl_b32 s9, s9, 13\n"
    "s_add_u32 s9, s9, s81\n"
    "v_mfma_f32_16x16x32_bf16 v[140:143], a[8:11], a[32:35], v[140:143]\n"
    "ds_read_b64_tr_b16 a[4:5], v5 offset:12288\n"
    "v_mfma_f32_16x16x32_bf16 v[172:175], a[8:11], a[36:39], v[172:175]\n"
    "s_add_u32 s17, s61, 6\n"
    "s_and_b32 s17, s17, 7\n"
    "ds_read_b64_tr_b16 a[6:7], v5 offset:12800\n"
    "v_mfma_f32_16x16x32_bf16 v[204:207], a[8:11], a[40:43], v[204:207]\n"
    "buffer_load_dwordx4 v11, s[36:39], s64 offen offset:3072 lds\n"
    "v_mfma_f32_16x16x32_bf16 v[236:239], a[8:11], a[44:47], v[236:239]\n"
    "s_waitcnt lgkmcnt(2)\n"
    "s_lshl_b32 s17, s17, 16\n"
    "s_add_u32 s17, s17, s79\n"
    "ds_read_b128 a[20:23], v2 offset:34816\n"
    "v_mfma_f32_16x16x32_bf16 v[144:147], a[0:3], a[32:35], v[144:147]\n"
    "s_xor_b32 s64, s64, 128\n"
    "s_add_u32 s64, s64, 262144\n"
    "s_and_b32 s64, s64, 0x3fffff\n"
    "s_add_u32 m0, s66, 8192\n"
    "ds_read_b64_tr_b16 a[8:9], v6 offset:12288\n"
    "v_mfma_f32_16x16x32_bf16 v[176:179], a[0:3], a[36:39], v[176:179]\n"
    "s_sub_u32 s10, s61, 7\n"
    "s_and_b32 s10, s10, 7\n"
    "ds_read_b64_tr_b16 a[10:11], v6 offset:12800\n"
    "v_mfma_f32_16x16x32_bf16 v[208:211], a[0:3], a[40:43], v[208:211]\n"
    "buffer_load_dwordx4 v8, s[36:39], s64 offen offset:0 lds\n"
    "v_mfma_f32_16x16x32_bf16 v[240:243], a[0:3], a[44:47], v[240:243]\n"
    "s_waitcnt lgkmcnt(3)\n"
    "s_lshl_b32 s10, s10, 13\n"
    "s_add_u32 s10, s10, s81\n"
    "ds_read_b128 a[24:27], v2 offset:36864\n"
    "v_mfma_f32_16x16x32_bf16 v[148:151], a[4:7], a[32:35], v[148:151]\n"
    "ds_read_b64_tr_b16 a[0:1], v7 offset:12288\n"
    "v_mfma_f32_16x16x32_bf16 v[180:183], a[4:7], a[36:39], v[180:183]\n"
    "s_add_u32 s18, s61, 7\n"
    "s_and_b32 s18, s18, 7\n"
    "ds_read_b64_tr_b16 a[2:3], v7 offset:12800\n"
    "v_mfma_f32_16x16x32_bf16 v[212:215], a[4:7], a[40:43], v[212:215]\n"
    "buffer_load_dwordx4 v9, s[36:39], s64 offen offset:1024 lds\n"
    "v_mfma_f32_16x16x32_bf16 v[244:247], a[4:7], a[44:47], v[244:247]\n"
    "s_waitcnt lgkmcnt(3)\n"
    "s_lshl_b32 s18, s18, 16\n"
    "s_add_u32 s18, s18, s79\n"
    "ds_read_b128 a[28:31], v2 offset:38912\n"
    "v_mfma_f32_16x16x32_bf16 v[152:155], a[8:11], a[32:35], v[152:155]\n"
    "s_cmp_eq_u32 s67, 7\n"
    "s_cbranch_scc1 L_w15_%=\n"
    "s_waitcnt vmcnt(6)\n"
    "L_w15_%=:\n"
    "s_waitcnt vmcnt(14)\n"
    "ds_read_b64_tr_b16 a[4:5], v4 offset:0\n"
    "v_mfma_f32_16x16x32_bf16 v[184:187], a[8:11], a[36:39], v[184:187]\n"
    "ds_read_b64_tr_b16 a[6:7], v4 offset:512\n"
    "v_mfma_f32_16x16x32_bf16 v[216:219], a[8:11], a[40:43], v[216:219]\n"
    "buffer_load_dwordx4 v10, s[36:39], s64 offen offset:2048 lds\n"
    "v_mfma_f32_16x16x32_bf16 v[248:251], a[8:11], a[44:47], v[248:251]\n"
    "s_waitcnt lgkmcnt(3)\n"
    "v_mfma_f32_16x16x32_bf16 v[156:159], a[0:3], a[32:35], v[156:159]\n"
    "ds_read_b64_tr_b16 a[8:9], v5 offset:0\n"
    "v_mfma_f32_16x16x32_bf16 v[188:191], a[0:3], a[36:39], v[188:191]\n"
    "ds_read_b64_tr_b16 a[10:11], v5 offset:512\n"
    "v_mfma_f32_16x16x32_bf16 v[220:223], a[0:3], a[40:43], v[220:223]\n"
    "buffer_load_dwordx4 v11, s[36:39], s64 offen offset:3072 lds\n"
    "v_mfma_f32_16x16x32_bf16 v[252:255], a[0:3], a[44:47], v[252:255]\n"
    "s_xor_b32 s64, s64, 128\n"
    "s_cmp_eq_u32 s70, 8\n"
    "s_cbranch_scc1 L_o8_%=\n"
    "L_b8_%=:\n"
    "s_waitcnt lgkmcnt(2)\n"
    "v_mfma_f32_16x16x32_bf16 v[128:131], a[4:7], a[16:19], v[128:131]\n"
    "s_add_u32 m0, s66, 12288\n"
    "ds_read_b64_tr_b16 a[0:1], v6 offset:0\n"
    "v_mfma_f32_16x16x32_bf16 v[160:163], a[4:7], a[20:23], v[160:163]\n"
    "ds_read_b64_tr_b16 a[2:3], v6 offset:512\n"
    "v_mfma_f32_16x16x32_bf16 v[192:195], a[4:7], a[24:27], v[192:195]\n"
    "buffer_load_dwordx4 v8, s[36:39], s64 offen offset:0 lds\n"
    "v_mfma_f32_16x16x32_bf16 v[224:227], a[4:7], a[28:31], v[224:227]\n"
    "s_waitcnt lgkmcnt(2)\n"
    "v_mfma_f32_16x16x32_bf16 v[132:135], a[8:11], a[16:19], v[132:135]\n"
    "ds_read_b64_tr_b16 a[4:5], v7 offset:0\n"
    "v_mfma_f32_16x16x32_bf16 v[164:167], a[8:11], a[20:23], v[164:167]\n"
    "ds_read_b64_tr_b16 a[6:7], v7 offset:512\n"
    "v_mfma_f32_16x16x32_bf16 v[196:199], a[8:11], a[24:27], v[196:199]\n"
    "buffer_load_dwordx4 v9, s[36:39], s64 offen offset:1024 lds\n"
    "v_mfma_f32_16x16x32_bf16 v[228:231], a[8:11], a[28:31], v[228:231]\n"
    "s_waitcnt lgkmcnt(2)\n"
    "ds_read_b128 a[32:35], v3 offset:32768\n"
    "v_mfma_f32_16x16x32_bf16 v[136:139], a[0:3], a[16:19], v[136:139]\n"
    "s_cmp_eq_u32 s67, 8\n"
    "s_cbranch_scc1 L_w16_%=\n"
    "s_waitcnt vmcnt(6)\n"
    "L_w16_%=:\n"
    "s_waitcnt vmcnt(14)\n"
    "ds_read_b64_tr_b16 a[8:9], v4 offset:4096\n"
    "v_mfma_f32_16x16x32_bf16 v[168:171], a[0:3], a[20:23], v[168:171]\n"
    "ds_read_b64_tr_b16 a[10:11], v4 offset:4608\n"
    "v_mfma_f32_16x16x32_bf16 v[200:203], a[0:3], a[24:27], v[200:203]\n"
    "buffer_load_dwordx4 v10, s[36:39], s64 offen offset:2048 lds\n"
    "v_mfma_f32_16x16x32_bf16 v[232:235], a[0:3], a[28:31], v[232:235]\n"
    "s_waitcnt lgkmcnt(3)\n"
    "v_mfma_f32_16x16x32_bf16 v[140:143], a[4:7], a[16:19], v[140:143]\n"
    "ds_read_b64_tr_b16 a[0:1], v5 offset:4096\n"
    "v_mfma_f32_16x16x32_bf16 v[172:175], a[4:7], a[20:23], v[172:175]\n"
    "ds_read_b64_tr_b16 a[2:3], v5 offset:4608\n"
    "v_mfma_f32_16x16x32_bf16 v[204:207], a[4:7], a[24:27], v[204:207]\n"
    "buffer_load_dwordx4 v11, s[36:39], s64 offen offset:3072 lds\n"
    "v_mfma_f32_16x16x32_bf16 v[236:239], a[4:7], a[28:31], v[236:239]\n"
    "s_waitcnt lgkmcnt(2)\n"
    "ds_read_b128 a[36:39], v3 offset:34816\n"
    "v_mfma_f32_16x16x32_bf16 v[144:147], a[8:11], a[16:19], v[144:147]\n"
    "s_xor_b32 s64, s64, 128\n"
    "s_add_u32 s64, s64, 262144\n"
    "s_and_b32 s64, s64, 0x3fffff\n"
    "s_add_u32 m0, s66, 0\n"
    "ds_read_b64_tr_b16 a[4:5], v6 offset:4096\n"
    "v_mfma_f32_16x16x32_bf16 v[176:179], a[8:11], a[20:23], v[176:179]\n"
    "ds_read_b64_tr_b16 a[6:7], v6 offset:4608\n"
    "v_mfma_f32_16x16x32_bf16 v[208:211], a[8:11], a[24:27], v[208:211]\n"
    "buffer_load_dwordx4 v8, s[36:39], s64 offen offset:0 lds\n"
    "v_mfma_f32_16x16x32_bf16 v[240:243], a[8:11], a[28:31], v[240:243]\n"
    "s_waitcnt lgkmcnt(3)\n"
    "ds_read_b128 a[40:43], v3 offset:36864\n"
    "v_mfma_f32_16x16x32_bf16 v[148:151], a[0:3], a[16:19], v[148:151]\n"
    "ds_read_b64_tr_b16 a[8:9], v7 offset:4096\n"
    "v_mfma_f32_16x16x32_bf16 v[180:183], a[0:3], a[20:23], v[180:183]\n"
    "ds_read_b64_tr_b16 a[10:11], v7 offset:4608\n"
    "v_mfma_f32_16x16x32_bf16 v[212:215], a[0:3], a[24:27], v[212:215]\n"
    "buffer_load_dwordx4 v9, s[36:39], s64 offen offset:1024 lds\n"
    "v_mfma_f32_16x16x32_bf16 v[244:247], a[0:3], a[28:31], v[244:247]\n"
    "s_waitcnt lgkmcnt(3)\n"
    "ds_read_b128 a[44:47], v3 offset:38912\n"
    "v_mfma_f32_16x16x32_bf16 v[152:155], a[4:7], a[16:19], v[152:155]\n"
    "s_cmp_eq_u32 s67, 8\n"
    "s_cbranch_scc1 L_w17_%=\n"
    "s_waitcnt vmcnt(6)\n"
    "L_w17_%=:\n"
    "s_waitcnt vmcnt(14)\n"
    "ds_read_b64_tr_b16 a[0:1], v4 offset:8192\n"
    "v_mfma_f32_16x16x32_bf16 v[184:187], a[4:7], a[20:23], v[184:187]\n"
    "ds_read_b64_tr_b16 a[2:3], v4 offset:8704\n"
    "v_mfma_f32_16x16x32_bf16 v[216:219], a[4:7], a[24:27], v[216:219]\n"
    "buffer_load_dwordx4 v10, s[36:39], s64 offen offset:2048 lds\n"
    "v_mfma_f32_16x16x32_bf16 v[248:251], a[4:7], a[28:31], v[248:251]\n"
    "s_waitcnt lgkmcnt(3)\n"
    "v_mfma_f32_16x16x32_bf16 v[156:159], a[8:11], a[16:19], v[156:159]\n"
    "ds_read_b64_tr_b16 a[4:5], v5 offset:8192\n"
    "v_mfma_f32_16x16x32_bf16 v[188:191], a[8:11], a[20:23], v[188:191]\n"
    "ds_read_b64_tr_b16 a[6:7], v5 offset:8704\n"
    "v_mfma_f32_16x16x32_bf16 v[220:223], a[8:11], a[24:27], v[220:223]\n"
    "buffer_load_dwordx4 v11, s[36:39], s64 offen offset:3072 lds\n"
    "v_mfma_f32_16x16x32_bf16 v[252:255], a[8:11], a[28:31], v[252:255]\n"
    "s_xor_b32 s64, s64, 128\n"
    "s_cmp_eq_u32 s70, 9\n"
    "s_cbranch_scc1 L_o9_%=\n"
    "L_b9_%=:\n"
    "s_waitcnt lgkmcnt(2)\n"
    "v_mfma_f32_16x16x32_bf16 v[128:131], a[0:3], a[32:35], v[128:131]\n"
    "s_add_u32 m0, s66, 4096\n"
    "ds_read_b64_tr_b16 a[8:9], v6 offset:8192\n"
    "v_mfma_f32_16x16x32_bf16 v[160:163], a[0:3], a[36:39], v[160:163]\n"
    "ds_read_b64_tr_b16 a[10:11], v6 offset:8704\n"
    "v_mfma_f32_16x16x32_bf16 v[192:195], a[0:3], a[40:43], v[192:195]\n"
    "buffer_load_dwordx4 v8, s[36:39], s64 offen offset:0 lds\n"
    "v_mfma_f32_16x16x32_bf16 v[224:227], a[0:3], a[44:47], v[224:227]\n"
    "s_waitcnt lgkmcnt(2)\n"
    "v_mfma_f32_16x16x32_bf16 v[132:135], a[4:7], a[32:35], v[132:135]\n"
    "ds_read_b64_tr_b16 a[0:1], v7 offset:8192\n"
    "v_mfma_f32_16x16x32_bf16 v[164:167], a[4:7], a[36:39], v[164:167]\n"
    "ds_read_b64_tr_b16 a[2:3], v7 offset:8704\n"
    "v_mfma_f32_16x16x32_bf16 v[196:199], a[4:7], a[40:43], v[196:199]\n"
    "buffer_load_dwordx4 v9, s[36:39], s64 offen offset:1024 lds\n"
    "v_mfma_f32_16x16x32_bf16 v[228:231], a[4:7], a[44:47], v[228:231]\n"
    "s_waitcnt lgkmcnt(2)\n"
    "ds_read_b128 a[16:19], v2 offset:40960\n"
    "v_mfma_f32_16x16x32_bf16 v[136:139], a[8:11], a[32:35], v[136:139]\n"
    "s_cmp_eq_u32 s67, 9\n"
    "s_cbranch_scc1 L_w18_%=\n"
    "s_waitcnt vmcnt(6)\n"
    "L_w18_%=:\n"
    "s_waitcnt vmcnt(14)\n"
    "ds_read_b64_tr_b16 a[4:5], v4 offset:12288\n"
    "v_mfma_f32_16x16x32_bf16 v[168:171], a[8:11], a[36:39], v[168:171]\n"
    "ds_read_b64_tr_b16 a[6:7], v4 offset:12800\n"
    "v_mfma_f32_16x16x32_bf16 v[200:203], a[8:11], a[40:43], v[200:203]\n"
    "buffer_load_dwordx4 v10, s[36:39], s64 offen offset:2048 lds\n"
    "v_mfma_f32_16x16x32_bf16 v[232:235], a[8:11], a[44:47], v[232:235]\n"
    "s_waitcnt lgkmcnt(3)\n"
    "v_mfma_f32_16x16x32_bf16 v[140:143], a[0:3], a[32:35], v[140:143]\n"
    "ds_read_b64_tr_b16 a[8:9], v5 offset:12288\n"
    "v_mfma_f32_16x16x32_bf16 v[172:175], a[0:3], a[36:39], v[172:175]\n"
    "ds_read_b64_tr_b16 a[10:11], v5 offset:12800\n"
    "v_mfma_f32_16x16x32_bf16 v[204:207], a[0:3], a[40:43], v[204:207]\n"
    "buffer_load_dwordx4 v11, s[36:39], s64 offen offset:3072 lds\n"
    "v_mfma_f32_16x16x32_bf16 v[236:239], a[0:3], a[44:47], v[236:239]\n"
    "s_waitcnt lgkmcnt(2)\n"
    "ds_read_b128 a[20:23], v2 offset:43008\n"
    "v_mfma_f32_16x16x32_bf16 v[144:147], a[4:7], a[32:35], v[144:147]\n"
    "s_xor_b32 s64, s64, 128\n"
    "s_add_u32 s64, s64, 262144\n"
    "s_and_b32 s64, s64, 0x3fffff\n"
    "s_add_u32 m0, s66, 8192\n"
    "ds_read_b64_tr_b16 a[0:1], v6 offset:12288\n"
    "v_mfma_f32_16x16x32_bf16 v[176:179], a[4:7], a[36:39], v[176:179]\n"
    "ds_read_b64_tr_b16 a[2:3], v6 offset:12800\n"
    "v_mfma_f32_16x16x32_bf16 v[208:211], a[4:7], a[40:43], v[208:211]\n"
    "buffer_load_dwordx4 v8, s[36:39], s64 offen offset:0 lds\n"
    "v_mfma_f32_16x16x32_bf16 v[240:243], a[4:7], a[44:47], v[240:243]\n"
    "s_waitcnt lgkmcnt(3)\n"
    "ds_read_b128 a[24:27], v2 offset:45056\n"
    "v_mfma_f32_16x16x32_bf16 v[148:151], a[8:11], a[32:35], v[148:151]\n"
    "ds_read_b64_tr_b16 a[4:5], v7 offset:12288\n"
    "v_mfma_f32_16x16x32_bf16 v[180:183], a[8:11], a[36:39], v[180:183]\n"
    "ds_read_b64_tr_b16 a[6:7], v7 offset:12800\n"
    "v_mfma_f32_16x16x32_bf16 v[212:215], a[8:11], a[40:43], v[212:215]\n"
    "buffer_load_dwordx4 v9, s[36:39], s64 offen offset:1024 lds\n"
    "v_mfma_f32_16x16x32_bf16 v[244:247], a[8:11], a[44:47], v[244:247]\n"
    "s_waitcnt lgkmcnt(3)\n"
    "ds_read_b128 a[28:31], v2 offset:47104\n"
    "v_mfma_f32_16x16x32_bf16 v[152:155], a[0:3], a[32:35], v[152:155]\n"
    "s_cmp_eq_u32 s67, 9\n"
    "s_cbranch_scc1 L_w19_%=\n"
    "s_waitcnt vmcnt(6)\n"
    "L_w19_%=:\n"
    "s_waitcnt vmcnt(14)\n"
    "ds_read_b64_tr_b16 a[8:9], v4 offset:0\n"
    "v_mfma_f32_16x16x32_bf16 v[184:187], a[0:3], a[36:39], v[184:187]\n"
    "ds_read_b64_tr_b16 a[10:11], v4 offset:512\n"
    "v_mfma_f32_16x16x32_bf16 v[216:219], a[0:3], a[40:43], v[216:219]\n"
    "buffer_load_dwordx4 v10, s[36:39], s64 offen offset:2048 lds\n"
    "v_mfma_f32_16x16x32_bf16 v[248:251], a[0:3], a[44:47], v[248:251]\n"
    "s_waitcnt lgkmcnt(3)\n"
    "v_mfma_f32_16x16x32_bf16 v[156:159], a[4:7], a[32:35], v[156:159]\n"
    "ds_read_b64_tr_b16 a[0:1], v5 offset:0\n"
    "v_mfma_f32_16x16x32_bf16 v[188:191], a[4:7], a[36:39], v[188:191]\n"
    "ds_read_b64_tr_b16 a[2:3], v5 offset:512\n"
    "v_mfma_f32_16x16x32_bf16 v[220:223], a[4:7], a[40:43], v[220:223]\n"
    "buffer_load_dwordx4 v11, s[36:39], s64 offen offset:3072 lds\n"
    "v_mfma_f32_16x16x32_bf16 v[252:255], a[4:7], a[44:47], v[252:255]\n"
    "s_xor_b32 s64, s64, 128\n"
    "s_cmp_eq_u32 s70, 10\n"
    "s_cbranch_scc1 L_o10_%=\n"
    "L_b10_%=:\n"
    "s_waitcnt lgkmcnt(2)\n"
    "v_mfma_f32_16x16x32_bf16 v[128:131], a[8:11], a[16:19], v[128:131]\n"
    "s_add_u32 m0, s66, 12288\n"
    "ds_read_b64_tr_b16 a[4:5], v6 offset:0\n"
    "v_mfma_f32_16x16x32_bf16 v[160:163], a[8:11], a[20:23], v[160:163]\n"
    "ds_read_b64_tr_b16 a[6:7], v6 offset:512\n"
    "v_mfma_f32_16x16x32_bf16 v[192:195], a[8:11], a[24:27], v[192:195]\n"
    "buffer_load_dwordx4 v8, s[36:39], s64 offen offset:0 lds\n"
    "v_mfma_f32_16x16x32_bf16 v[224:227], a[8:11], a[28:31], v[224:227]\n"
    "s_waitcnt lgkmcnt(2)\n"
    "v_mfma_f32_16x16x32_bf16 v[132:135], a[0:3], a[16:19], v[132:135]\n"
    "ds_read_b64_tr_b16 a[8:9], v7 offset:0\n"
    "v_mfma_f32_16x16x32_bf16 v[164:167], a[0:3], a[20:23], v[164:167]\n"
    "ds_read_b64_tr_b16 a[10:11], v7 offset:512\n"
    "v_mfma_f32_16x16x32_bf16 v[196:199], a[0:3], a[24:27], v[196:199]\n"
    "buffer_load_dwordx4 v9, s[36:39], s64 offen offset:1024 lds\n"
    "v_mfma_f32_16x16x32_bf16 v[228:231], a[0:3], a[28:31], v[228:231]\n"
    "s_waitcnt lgkmcnt(2)\n"
    "ds_read_b128 a[32:35], v3 offset:40960\n"
    "v_mfma_f32_16x16x32_bf16 v[136:139], a[4:7], a[16:19], v[136:139]\n"
    "s_cmp_eq_u32 s67, 10\n"
    "s_cbranch_scc1 L_w20_%=\n"
    "s_waitcnt vmcnt(6)\n"
    "L_w20_%=:\n"
    "s_waitcnt vmcnt(14)\n"
    "ds_read_b64_tr_b16 a[0:1], v4 offset:4096\n"
    "v_mfma_f32_16x16x32_bf16 v[168:171], a[4:7], a[20:23], v[168:171]\n"
    "ds_read_b64_tr_b16 a[2:3], v4 offset:4608\n"
    "v_mfma_f32_16x16x32_bf16 v[200:203], a[4:7], a[24:27], v[200:203]\n"
    "buffer_load_dwordx4 v10, s[36:39], s64 offen offset:2048 lds\n"
    "v_mfma_f32_16x16x32_bf16 v[232:235], a[4:7], a[28:31], v[232:235]\n"
    "s_waitcnt lgkmcnt(3)\n"
    "v_mfma_f32_16x16x32_bf16 v[140:143], a[8:11], a[16:19], v[140:143]\n"
    "ds_read_b64_tr_b16 a[4:5], v5 offset:4096\n"
    "v_mfma_f32_16x16x32_bf16 v[172:175], a[8:11], a[20:23], v[172:175]\n"
    "ds_read_b64_tr_b16 a[6:7], v5 offset:4608\n"
    "v_mfma_f32_16x16x32_bf16 v[204:207], a[8:11], a[24:27], v[204:207]\n"
    "buffer_load_dwordx4 v11, s[36:39], s64 offen offset:3072 lds\n"
    "v_mfma_f32_16x16x32_bf16 v[236:239], a[8:11], a[28:31], v[236:239]\n"
    "s_waitcnt lgkmcnt(2)\n"
    "ds_read_b128 a[36:39], v3 offset:43008\n"
    "v_mfma_f32_16x16x32_bf16 v[144:147], a[0:3], a[16:19], v[144:147]\n"
    "s_xor_b32 s64, s64, 128\n"
    "s_add_u32 s64, s64, 262144\n"
    "s_and_b32 s64, s64, 0x3fffff\n"
    "s_add_u32 m0, s66, 0\n"
    "ds_read_b64_tr_b16 a[8:9], v6 offset:4096\n"
    "v_mfma_f32_16x16x32_bf16 v[176:179], a[0:3], a[20:23], v[176:179]\n"
    "ds_read_b64_tr_b16 a[10:11], v6 offset:4608\n"
    "v_mfma_f32_16x16x32_bf16 v[208:211], a[0:3], a[24:27], v[208:211]\n"
    "buffer_load_dwordx4 v8, s[36:39], s64 offen offset:0 lds\n"
    "v_mfma_f32_16x16x32_bf16 v[240:243], a[0:3], a[28:31], v[240:243]\n"
    "s_waitcnt lgkmcnt(3)\n"
    "ds_read_b128 a[40:43], v3 offset:45056\n"
    "v_mfma_f32_16x16x32_bf16 v[148:151], a[4:7], a[16:19], v[148:151]\n"
    "ds_read_b64_tr_b16 a[0:1], v7 offset:4096\n"
    "v_mfma_f32_16x16x32_bf16 v[180:183], a[4:7], a[20:23], v[180:183]\n"
    "ds_read_b64_tr_b16 a[2:3], v7 offset:4608\n"
    "v_mfma_f32_16x16x32_bf16 v[212:215], a[4:7], a[24:27], v[212:215]\n"
    "buffer_load_dwordx4 v9, s[36:39], s64 offen offset:1024 lds\n"
    "v_mfma_f32_16x16x32_bf16 v[244:247], a[4:7], a[28:31], v[244:247]\n"
    "s_waitcnt lgkmcnt(3)\n"
    "ds_read_b128 a[44:47], v3 offset:47104\n"
    "v_mfma_f32_16x16x32_bf16 v[152:155], a[8:11], a[16:19], v[152:155]\n"
    "s_cmp_eq_u32 s67, 10\n"
    "s_cbranch_scc1 L_w21_%=\n"
    "s_waitcnt vmcnt(6)\n"
    "L_w21_%=:\n"
    "s_waitcnt vmcnt(14)\n"
    "ds_read_b64_tr_b16 a[4:5], v4 offset:8192\n"
    "v_mfma_f32_16x16x32_bf16 v[184:187], a[8:11], a[20:23], v[184:187]\n"
    "ds_read_b64_tr_b16 a[6:7], v4 offset:8704\n"
    "v_mfma_f32_16x16x32_bf16 v[216:219], a[8:11], a[24:27], v[216:219]\n"
    "buffer_load_dwordx4 v10, s[36:39], s64 offen offset:2048 lds\n"
    "v_mfma_f32_16x16x32_bf16 v[248:251], a[8:11], a[28:31], v[248:251]\n"
    "s_waitcnt lgkmcnt(3)\n"
    "v_mfma_f32_16x16x32_bf16 v[156:159], a[0:3], a[16:19], v[156:159]\n"
    "ds_read_b64_tr_b16 a[8:9], v5 offset:8192\n"
    "v_mfma_f32_16x16x32_bf16 v[188:191], a[0:3], a[20:23], v[188:191]\n"
    "ds_read_b64_tr_b16 a[10:11], v5 offset:8704\n"
    "v_mfma_f32_16x16x32_bf16 v[220:223], a[0:3], a[24:27], v[220:223]\n"
    "buffer_load_dwordx4 v11, s[36:39], s64 offen offset:3072 lds\n"
    "v_mfma_f32_16x16x32_bf16 v[252:255], a[0:3], a[28:31], v[252:255]\n"
    "s_xor_b32 s64, s64, 128\n"
    "s_cmp_eq_u32 s70, 11\n"
    "s_cbranch_scc1 L_o11_%=\n"
    "L_b11_%=:\n"
    "s_waitcnt lgkmcnt(2)\n"
    "v_mfma_f32_16x16x32_bf16 v[128:131], a[4:7], a[32:35], v[128:131]\n"
    "s_add_u32 m0, s66, 4096\n"
    "ds_read_b64_tr_b16 a[0:1], v6 offset:8192\n"
    "v_mfma_f32_16x16x32_bf16 v[160:163], a[4:7], a[36:39], v[160:163]\n"
    "ds_read_b64_tr_b16 a[2:3], v6 offset:8704\n"
    "v_mfma_f32_16x16x32_bf16 v[192:195], a[4:7], a[40:43], v[192:195]\n"
    "buffer_load_dwordx4 v8, s[36:39], s64 offen offset:0 lds\n"
    "v_mfma_f32_16x16x32_bf16 v[224:227], a[4:7], a[44:47], v[224:227]\n"
    "s_waitcnt lgkmcnt(2)\n"
    "v_mfma_f32_16x16x32_bf16 v[132:135], a[8:11], a[32:35], v[132:135]\n"
    "ds_read_b64_tr_b16 a[4:5], v7 offset:8192\n"
    "v_mfma_f32_16x16x32_bf16 v[164:167], a[8:11], a[36:39], v[164:167]\n"
    "ds_read_b64_tr_b16 a[6:7], v7 offset:8704\n"
    "v_mfma_f32_16x16x32_bf16 v[196:199], a[8:11], a[40:43], v[196:199]\n"
    "buffer_load_dwordx4 v9, s[36:39], s64 offen offset:1024 lds\n"
    "v_mfma_f32_16x16x32_bf16 v[228:231], a[8:11], a[44:47], v[228:231]\n"
    "s_waitcnt lgkmcnt(2)\n"
    "ds_read_b128 a[16:19], v2 offset:49152\n"
    "v_mfma_f32_16x16x32_bf16 v[136:139], a[0:3], a[32:35], v[136:139]\n"
    "s_cmp_eq_u32 s67, 11\n"
    "s_cbranch_scc1 L_w22_%=\n"
    "s_waitcnt vmcnt(6)\n"
    "L_w22_%=:\n"
    "s_waitcnt vmcnt(14)\n"
    "ds_read_b64_tr_b16 a[8:9], v4 offset:12288\n"
    "v_mfma_f32_16x16x32_bf16 v[168:171], a[0:3], a[36:39], v[168:171]\n"
    "ds_read_b64_tr_b16 a[10:11], v4 offset:12800\n"
    "v_mfma_f32_16x16x32_bf16 v[200:203], a[0:3], a[40:43], v[200:203]\n"
    "buffer_load_dwordx4 v10, s[36:39], s64 offen offset:2048 lds\n"
    "v_mfma_f32_16x16x32_bf16 v[232:235], a[0:3], a[44:47], v[232:235]\n"
    "s_waitcnt lgkmcnt(3)\n"
    "v_mfma_f32_16x16x32_bf16 v[140:143], a[4:7], a[32:35], v[140:143]\n"
    "ds_read_b64_tr_b16 a[0:1], v5 offset:12288\n"
    "v_mfma_f32_16x16x32_bf16 v[172:175], a[4:7], a[36:39], v[172:175]\n"
    "ds_read_b64_tr_b16 a[2:3], v5 offset:12800\n"
    "v_mfma_f32_16x16x32_bf16 v[204:207], a[4:7], a[40:43], v[204:207]\n"
    "buffer_load_dwordx4 v11, s[36:39], s64 offen offset:3072 lds\n"
    "v_mfma_f32_16x16x32_bf16 v[236:239], a[4:7], a[44:47], v[236:239]\n"
    "s_waitcnt lgkmcnt(2)\n"
    "ds_read_b128 a[20:23], v2 offset:51200\n"
    "v_mfma_f32_16x16x32_bf16 v[144:147], a[8:11], a[32:35], v[144:147]\n"
    "s_xor_b32 s64, s64, 128\n"
    "s_add_u32 s64, s64, 262144\n"
    "s_and_b32 s64, s64, 0x3fffff\n"
    "s_add_u32 m0, s66, 8192\n"
    "ds_read_b64_tr_b16 a[4:5], v6 offset:12288\n"
    "v_mfma_f32_16x16x32_bf16 v[176:179], a[8:11], a[36:39], v[176:179]\n"
    "ds_read_b64_tr_b16 a[6:7], v6 offset:12800\n"
    "v_mfma_f32_16x16x32_bf16 v[208:211], a[8:11], a[40:43], v[208:211]\n"
    "buffer_load_dwordx4 v8, s[36:39], s64 offen offset:0 lds\n"
    "v_mfma_f32_16x16x32_bf16 v[240:243], a[8:11], a[44:47], v[240:243]\n"
    "s_waitcnt lgkmcnt(3)\n"
    "ds_read_b128 a[24:27], v2 offset:53248\n"
    "v_mfma_f32_16x16x32_bf16 v[148:151], a[0:3], a[32:35], v[148:151]\n"
    "ds_read_b64_tr_b16 a[8:9], v7 offset:12288\n"
    "v_mfma_f32_16x16x32_bf16 v[180:183], a[0:3], a[36:39], v[180:183]\n"
    "ds_read_b64_tr_b16 a[10:11], v7 offset:12800\n"
    "v_mfma_f32_16x16x32_bf16 v[212:215], a[0:3], a[40:43], v[212:215]\n"
    "buffer_load_dwordx4 v9, s[36:39], s64 offen offset:1024 lds\n"
    "v_mfma_f32_16x16x32_bf16 v[244:247], a[0:3], a[44:47], v[244:247]\n"
    "s_waitcnt lgkmcnt(3)\n"
    "ds_read_b128 a[28:31], v2 offset:55296\n"
    "v_mfma_f32_16x16x32_bf16 v[152:155], a[4:7], a[32:35], v[152:155]\n"
    "s_cmp_eq_u32 s67, 11\n"
    "s_cbranch_scc1 L_w23_%=\n"
    "s_waitcnt vmcnt(6)\n"
    "L_w23_%=:\n"
    "s_waitcnt vmcnt(14)\n"
    "ds_read_b64_tr_b16 a[0:1], v4 offset:0\n"
    "v_mfma_f32_16x16x32_bf16 v[184:187], a[4:7], a[36:39], v[184:187]\n"
    "ds_read_b64_tr_b16 a[2:3], v4 offset:512\n"
    "v_mfma_f32_16x16x32_bf16 v[216:219], a[4:7], a[40:43], v[216:219]\n"
    "buffer_load_dwordx4 v10, s[36:39], s64 offen offset:2048 lds\n"
    "v_mfma_f32_16x16x32_bf16 v[248:251], a[4:7], a[44:47], v[248:251]\n"
    "s_waitcnt lgkmcnt(3)\n"
    "v_mfma_f32_16x16x32_bf16 v[156:159], a[8:11], a[32:35], v[156:159]\n"
    "ds_read_b64_tr_b16 a[4:5], v5 offset:0\n"
    "v_mfma_f32_16x16x32_bf16 v[188:191], a[8:11], a[36:39], v[188:191]\n"
    "ds_read_b64_tr_b16 a[6:7], v5 offset:512\n"
    "v_mfma_f32_16x16x32_bf16 v[220:223], a[8:11], a[40:43], v[220:223]\n"
    "buffer_load_dwordx4 v11, s[36:39], s64 offen offset:3072 lds\n"
    "v_mfma_f32_16x16x32_bf16 v[252:255], a[8:11], a[44:47], v[252:255]\n"
    "s_waitcnt lgkmcnt(2)\n"
    "s_xor_b32 s64, s64, 128\n"
    "v_mfma_f32_16x16x32_bf16 v[128:131], a[0:3], a[16:19], v[128:131]\n"
    "s_add_u32 m0, s66, 12288\n"
    "ds_read_b64_tr_b16 a[8:9], v6 offset:0\n"
    "v_mfma_f32_16x16x32_bf16 v[160:163], a[0:3], a[20:23], v[160:163]\n"
    "ds_read_b64_tr_b16 a[10:11], v6 offset:512\n"
    "v_mfma_f32_16x16x32_bf16 v[192:195], a[0:3], a[24:27], v[192:195]\n"
    "buffer_load_dwordx4 v8, s[36:39], s64 offen offset:0 lds\n"
    "v_mfma_f32_16x16x32_bf16 v[224:227], a[0:3], a[28:31], v[224:227]\n"
    "s_waitcnt lgkmcnt(2)\n"
    "v_mfma_f32_16x16x32_bf16 v[132:135], a[4:7], a[16:19], v[132:135]\n"
    "ds_read_b64_tr_b16 a[0:1], v7 offset:0\n"
    "v_mfma_f32_16x16x32_bf16 v[164:167], a[4:7], a[20:23], v[164:167]\n"
    "ds_read_b64_tr_b16 a[2:3], v7 offset:512\n"
    "v_mfma_f32_16x16x32_bf16 v[196:199], a[4:7], a[24:27], v[196:199]\n"
    "buffer_load_dwordx4 v9, s[36:39], s64 offen offset:1024 lds\n"
    "v_mfma_f32_16x16x32_bf16 v[228:231], a[4:7], a[28:31], v[228:231]\n"
    "s_waitcnt lgkmcnt(2)\n"
    "ds_read_b128 a[32:35], v3 offset:49152\n"
    "v_mfma_f32_16x16x32_bf16 v[136:139], a[8:11], a[16:19], v[136:139]\n"
    "s_waitcnt vmcnt(6)\n"
    "ds_read_b64_tr_b16 a[4:5], v4 offset:4096\n"
    "v_mfma_f32_16x16x32_bf16 v[168:171], a[8:11], a[20:23], v[168:171]\n"
    "ds_read_b64_tr_b16 a[6:7], v4 offset:4608\n"
    "v_mfma_f32_16x16x32_bf16 v[200:203], a[8:11], a[24:27], v[200:203]\n"
    "buffer_load_dwordx4 v10, s[36:39], s64 offen offset:2048 lds\n"
    "v_mfma_f32_16x16x32_bf16 v[232:235], a[8:11], a[28:31], v[232:235]\n"
    "s_waitcnt lgkmcnt(3)\n"
    "v_mfma_f32_16x16x32_bf16 v[140:143], a[0:3], a[16:19], v[140:143]\n"
    "ds_read_b64_tr_b16 a[8:9], v5 offset:4096\n"
    "v_mfma_f32_16x16x32_bf16 v[172:175], a[0:3], a[20:23], v[172:175]\n"
    "ds_read_b64_tr_b16 a[10:11], v5 offset:4608\n"
    "v_mfma_f32_16x16x32_bf16 v[204:207], a[0:3], a[24:27], v[204:207]\n"
    "buffer_load_dwordx4 v11, s[36:39], s64 offen offset:3072 lds\n"
    "v_mfma_f32_16x16x32_bf16 v[236:239], a[0:3], a[28:31], v[236:239]\n"
    "s_waitcnt lgkmcnt(2)\n"
    "ds_read_b128 a[36:39], v3 offset:51200\n"
    "v_mfma_f32_16x16x32_bf16 v[144:147], a[4:7], a[16:19], v[144:147]\n"
    "s_xor_b32 s64, s64, 128\n"
    "s_add_u32 s64, s64, 262144\n"
    "s_and_b32 s64, s64, 0x3fffff\n"
    "s_add_u32 m0, s66, 0\n"
    "ds_read_b64_tr_b16 a[0:1], v6 offset:4096\n"
    "v_mfma_f32_16x16x32_bf16 v[176:179], a[4:7], a[20:23], v[176:179]\n"
    "ds_read_b64_tr_b16 a[2:3], v6 offset:4608\n"
    "v_mfma_f32_16x16x32_bf16 v[208:211], a[4:7], a[24:27], v[208:211]\n"
    "buffer_load_dwordx4 v8, s[36:39], s64 offen offset:0 lds\n"
    "v_mfma_f32_16x16x32_bf16 v[240:243], a[4:7], a[28:31], v[240:243]\n"
    "s_waitcnt lgkmcnt(3)\n"
    "ds_read_b128 a[40:43], v3 offset:53248\n"
    "ds_read_b64_tr_b16 a[192:193], v4 offset:16384\n"
    "v_mfma_f32_16x16x32_bf16 v[148:151], a[8:11], a[16:19], v[148:151]\n"
    "ds_read_b64_tr_b16 a[4:5], v7 offset:4096\n"
    "v_mfma_f32_16x16x32_bf16 v[180:183], a[8:11], a[20:23], v[180:183]\n"
    "ds_read_b64_tr_b16 a[6:7], v7 offset:4608\n"
    "v_mfma_f32_16x16x32_bf16 v[212:215], a[8:11], a[24:27], v[212:215]\n"
    "buffer_load_dwordx4 v9, s[36:39], s64 offen offset:1024 lds\n"
    "v_mfma_f32_16x16x32_bf16 v[244:247], a[8:11], a[28:31], v[244:247]\n"
    "s_waitcnt lgkmcnt(4)\n"
    "ds_read_b128 a[44:47], v3 offset:55296\n"
    "ds_read_b64_tr_b16 a[194:195], v4 offset:16896\n"
    "v_mfma_f32_16x16x32_bf16 v[152:155], a[0:3], a[16:19], v[152:155]\n"
    "s_waitcnt vmcnt(6)\n"
    "ds_read_b64_tr_b16 a[8:9], v4 offset:8192\n"
    "v_mfma_f32_16x16x32_bf16 v[184:187], a[0:3], a[20:23], v[184:187]\n"
    "ds_read_b64_tr_b16 a[10:11], v4 offset:8704\n"
    "v_mfma_f32_16x16x32_bf16 v[216:219], a[0:3], a[24:27], v[216:219]\n"
    "buffer_load_dwordx4 v10, s[36:39], s64 offen offset:2048 lds\n"
    "v_mfma_f32_16x16x32_bf16 v[248:251], a[0:3], a[28:31], v[248:251]\n"
    "s_waitcnt lgkmcnt(4)\n"
    "ds_read_b64_tr_b16 a[196:197], v5 offset:16384\n"
    "v_mfma_f32_16x16x32_bf16 v[156:159], a[4:7], a[16:19], v[156:159]\n"
    "ds_read_b64_tr_b16 a[0:1], v5 offset:8192\n"
    "v_mfma_f32_16x16x32_bf16 v[188:191], a[4:7], a[20:23], v[188:191]\n"
    "ds_read_b64_tr_b16 a[2:3], v5 offset:8704\n"
    "v_mfma_f32_16x16x32_bf16 v[220:223], a[4:7], a[24:27], v[220:223]\n"
    "buffer_load_dwordx4 v11, s[36:39], s64 offen offset:3072 lds\n"
    "v_mfma_f32_16x16x32_bf16 v[252:255], a[4:7], a[28:31], v[252:255]\n"
    "ds_read_b64_tr_b16 a[198:199], v5 offset:16896\n"
    "s_xor_b32 s64, s64, 128\n"
    "v_readlane_b32 s80, v23, 7\n"
    "L_nonce_%=:\n"
    "s_nop 1\n"
    "s_add_u32 s80, s80, 1\n"
    "v_cmp_eq_u32 vcc, s80, v23\n"
    "s_nop 1\n"
    "s_cbranch_vccnz L_nonce_%=\n"
    "v_mov_b32 v28, s80\n"
    "s_waitcnt lgkmcnt(4)\n"
    "v_mfma_f32_16x16x32_bf16 v[128:131], a[8:11], a[32:35], v[128:131]\n"
    "s_add_u32 m0, s66, 4096\n"
    "ds_read_b64_tr_b16 a[4:5], v6 offset:8192\n"
    "v_mfma_f32_16x16x32_bf16 v[160:163], a[8:11], a[36:39], v[160:163]\n"
    "ds_read_b64_tr_b16 a[6:7], v6 offset:8704\n"
    "v_mfma_f32_16x16x32_bf16 v[192:195], a[8:11], a[40:43], v[192:195]\n"
    "buffer_load_dwordx4 v8, s[36:39], s64 offen offset:0 lds\n"
    "v_mfma_f32_16x16x32_bf16 v[224:227], a[8:11], a[44:47], v[224:227]\n"
    "s_waitcnt lgkmcnt(3)\n"
    "ds_read_b64_tr_b16 a[200:201], v6 offset:16384\n"
    "v_mfma_f32_16x16x32_bf16 v[132:135], a[0:3], a[32:35], v[132:135]\n"
    "ds_read_b64_tr_b16 a[8:9], v7 offset:8192\n"
    "v_mfma_f32_16x16x32_bf16 v[164:167], a[0:3], a[36:39], v[164:167]\n"
    "ds_read_b64_tr_b16 a[10:11], v7 offset:8704\n"
    "v_mfma_f32_16x16x32_bf16 v[196:199], a[0:3], a[40:43], v[196:199]\n"
    "buffer_load_dwordx4 v9, s[36:39], s64 offen offset:1024 lds\n"
    "v_mfma_f32_16x16x32_bf16 v[228:231], a[0:3], a[44:47], v[228:231]\n"
    "s_waitcnt lgkmcnt(3)\n"
    "ds_read_b128 a[16:19], v2 offset:57344\n"
    "ds_read_b64_tr_b16 a[202:203], v6 offset:16896\n"
    "v_mfma_f32_16x16x32_bf16 v[136:139], a[4:7], a[32:35], v[136:139]\n"
    "s_waitcnt vmcnt(6)\n"
    "ds_read_b64_tr_b16 a[0:1], v4 offset:12288\n"
    "v_mfma_f32_16x16x32_bf16 v[168:171], a[4:7], a[36:39], v[168:171]\n"
    "ds_read_b64_tr_b16 a[2:3], v4 offset:12800\n"
    "v_mfma_f32_16x16x32_bf16 v[200:203], a[4:7], a[40:43], v[200:203]\n"
    "buffer_load_dwordx4 v10, s[36:39], s64 offen offset:2048 lds\n"
    "v_mfma_f32_16x16x32_bf16 v[232:235], a[4:7], a[44:47], v[232:235]\n"
    "s_waitcnt lgkmcnt(4)\n"
    "ds_read_b64_tr_b16 a[204:205], v7 offset:16384\n"
    "v_mfma_f32_16x16x32_bf16 v[140:143], a[8:11], a[32:35], v[140:143]\n"
    "ds_read_b64_tr_b16 a[4:5], v5 offset:12288\n"
    "v_mfma_f32_16x16x32_bf16 v[172:175], a[8:11], a[36:39], v[172:175]\n"
    "ds_read_b64_tr_b16 a[6:7], v5 offset:12800\n"
    "v_mfma_f32_16x16x32_bf16 v[204:207], a[8:11], a[40:43], v[204:207]\n"
    "buffer_load_dwordx4 v11, s[36:39], s64 offen offset:3072 lds\n"
    "v_mfma_f32_16x16x32_bf16 v[236:239], a[8:11], a[44:47], v[236:239]\n"
    "s_waitcnt lgkmcnt(3)\n"
    "ds_read_b128 a[20:23], v2 offset:59392\n"
    "ds_read_b64_tr_b16 a[206:207], v7 offset:16896\n"
    "v_mfma_f32_16x16x32_bf16 v[144:147], a[0:3], a[32:35], v[144:147]\n"
    "s_xor_b32 s64, s64, 128\n"
    "s_add_u32 s64, s64, 262144\n"
    "s_and_b32 s64, s64, 0x3fffff\n"
    "s_add_u32 m0, s66, 8192\n"
    "ds_read_b64_tr_b16 a[8:9], v6 offset:12288\n"
    "v_mfma_f32_16x16x32_bf16 v[176:179], a[0:3], a[36:39], v[176:179]\n"
    "ds_read_b64_tr_b16 a[10:11], v6 offset:12800\n"
    "v_mfma_f32_16x16x32_bf16 v[208:211], a[0:3], a[40:43], v[208:211]\n"
    "buffer_load_dwordx4 v8, s[36:39], s64 offen offset:0 lds\n"
    "v_mfma_f32_16x16x32_bf16 v[240:243], a[0:3], a[44:47], v[240:243]\n"
    "s_waitcnt lgkmcnt(4)\n"
    "ds_read_b128 a[24:27], v2 offset:61440\n"
    "ds_read_b64_tr_b16 a[208:209], v4 offset:20480\n"
    "v_mfma_f32_16x16x32_bf16 v[148:151], a[4:7], a[32:35], v[148:151]\n"
    "ds_read_b64_tr_b16 a[0:1], v7 offset:12288\n"
    "v_mfma_f32_16x16x32_bf16 v[180:183], a[4:7], a[36:39], v[180:183]\n"
    "ds_read_b64_tr_b16 a[2:3], v7 offset:12800\n"
    "v_mfma_f32_16x16x32_bf16 v[212:215], a[4:7], a[40:43], v[212:215]\n"
    "buffer_load_dwordx4 v9, s[36:39], s64 offen offset:1024 lds\n"
    "v_mfma_f32_16x16x32_bf16 v[244:247], a[4:7], a[44:47], v[244:247]\n"
    "s_waitcnt lgkmcnt(4)\n"
    "ds_read_b128 a[28:31], v2 offset:63488\n"
    "ds_read_b64_tr_b16 a[210:211], v4 offset:20992\n"
    "v_mfma_f32_16x16x32_bf16 v[152:155], a[8:11], a[32:35], v[152:155]\n"
    "s_waitcnt vmcnt(6)\n"
    "ds_read_b64_tr_b16 a[4:5], v4 offset:0\n"
    "v_mfma_f32_16x16x32_bf16 v[184:187], a[8:11], a[36:39], v[184:187]\n"
    "ds_read_b64_tr_b16 a[6:7], v4 offset:512\n"
    "v_mfma_f32_16x16x32_bf16 v[216:219], a[8:11], a[40:43], v[216:219]\n"
    "buffer_load_dwordx4 v10, s[36:39], s64 offen offset:2048 lds\n"
    "v_mfma_f32_16x16x32_bf16 v[248:251], a[8:11], a[44:47], v[248:251]\n"
    "s_waitcnt lgkmcnt(4)\n"
    "ds_read_b64_tr_b16 a[212:213], v5 offset:20480\n"
    "v_mfma_f32_16x16x32_bf16 v[156:159], a[0:3], a[32:35], v[156:159]\n"
    "ds_read_b64_tr_b16 a[8:9], v5 offset:0\n"
    "v_mfma_f32_16x16x32_bf16 v[188:191], a[0:3], a[36:39], v[188:191]\n"
    "ds_read_b64_tr_b16 a[10:11], v5 offset:512\n"
    "v_mfma_f32_16x16x32_bf16 v[220:223], a[0:3], a[40:43], v[220:223]\n"
    "buffer_load_dwordx4 v11, s[36:39], s64 offen offset:3072 lds\n"
    "v_mfma_f32_16x16x32_bf16 v[252:255], a[0:3], a[44:47], v[252:255]\n"
    "s_waitcnt lgkmcnt(3)\n"
    "ds_read_b64_tr_b16 a[214:215], v5 offset:20992\n"
    "s_xor_b32 s64, s64, 128\n"
    "v_mfma_f32_16x16x32_bf16 v[128:131], a[4:7], a[16:19], v[128:131]\n"
    "s_add_u32 m0, s66, 12288\n"
    "ds_read_b64_tr_b16 a[0:1], v6 offset:0\n"
    "v_mfma_f32_16x16x32_bf16 v[160:163], a[4:7], a[20:23], v[160:163]\n"
    "ds_read_b64_tr_b16 a[2:3], v6 offset:512\n"
    "v_mfma_f32_16x16x32_bf16 v[192:195], a[4:7], a[24:27], v[192:195]\n"
    "buffer_load_dwordx4 v8, s[36:39], s64 offen offset:0 lds\n"
    "v_mfma_f32_16x16x32_bf16 v[224:227], a[4:7], a[28:31], v[224:227]\n"
    "s_waitcnt lgkmcnt(3)\n"
    "ds_read_b64_tr_b16 a[216:217], v6 offset:20480\n"
    "v_mfma_f32_16x16x32_bf16 v[132:135], a[8:11], a[16:19], v[132:135]\n"
    "ds_read_b64_tr_b16 a[4:5], v7 offset:0\n"
    "v_mfma_f32_16x16x32_bf16 v[164:167], a[8:11], a[20:23], v[164:167]\n"
    "ds_read_b64_tr_b16 a[6:7], v7 offset:512\n"
    "v_mfma_f32_16x16x32_bf16 v[196:199], a[8:11], a[24:27], v[196:199]\n"
    "buffer_load_dwordx4 v9, s[36:39], s64 offen offset:1024 lds\n"
    "v_mfma_f32_16x16x32_bf16 v[228:231], a[8:11], a[28:31], v[228:231]\n"
    "s_waitcnt lgkmcnt(3)\n"
    "ds_read_b128 a[32:35], v3 offset:57344\n"
    "ds_read_b64_tr_b16 a[218:219], v6 offset:20992\n"
    "v_mfma_f32_16x16x32_bf16 v[136:139], a[0:3], a[16:19], v[136:139]\n"
    "s_waitcnt vmcnt(6)\n"
    "ds_read_b64_tr_b16 a[8:9], v4 offset:4096\n"
    "v_mfma_f32_16x16x32_bf16 v[168:171], a[0:3], a[20:23], v[168:171]\n"
    "ds_read_b64_tr_b16 a[10:11], v4 offset:4608\n"
    "v_mfma_f32_16x16x32_bf16 v[200:203], a[0:3], a[24:27], v[200:203]\n"
    "buffer_load_dwordx4 v10, s[36:39], s64 offen offset:2048 lds\n"
    "v_mfma_f32_16x16x32_bf16 v[232:235], a[0:3], a[28:31], v[232:235]\n"
    "s_waitcnt lgkmcnt(4)\n"
    "ds_read_b64_tr_b16 a[220:221], v7 offset:20480\n"
    "v_mfma_f32_16x16x32_bf16 v[140:143], a[4:7], a[16:19], v[140:143]\n"
    "ds_read_b64_tr_b16 a[0:1], v5 offset:4096\n"
    "v_mfma_f32_16x16x32_bf16 v[172:175], a[4:7], a[20:23], v[172:175]\n"
    "ds_read_b64_tr_b16 a[2:3], v5 offset:4608\n"
    "v_mfma_f32_16x16x32_bf16 v[204:207], a[4:7], a[24:27], v[204:207]\n"
    "buffer_load_dwordx4 v11, s[36:39], s64 offen offset:3072 lds\n"
    "v_mfma_f32_16x16x32_bf16 v[236:239], a[4:7], a[28:31], v[236:239]\n"
    "s_waitcnt lgkmcnt(3)\n"
    "ds_read_b128 a[36:39], v3 offset:59392\n"
    "ds_read_b64_tr_b16 a[222:223], v7 offset:20992\n"
    "v_mfma_f32_16x16x32_bf16 v[144:147], a[8:11], a[16:19], v[144:147]\n"
    "s_xor_b32 s64, s64, 128\n"
    "s_add_u32 s64, s64, 262144\n"
    "s_and_b32 s64, s64, 0x3fffff\n"
    "s_add_u32 m0, s66, 16384\n"
    "s_xor_b32 s74, s26, 8192\n"
    "ds_read_b64_tr_b16 a[4:5], v6 offset:4096\n"
    "v_mfma_f32_16x16x32_bf16 v[176:179], a[8:11], a[20:23], v[176:179]\n"
    "ds_read_b64_tr_b16 a[6:7], v6 offset:4608\n"
    "v_mfma_f32_16x16x32_bf16 v[208:211], a[8:11], a[24:27], v[208:211]\n"
    "s_waitcnt lgkmcnt(2)\n"
    "buffer_load_dwordx4 v64, s[44:47], s74 offen offset:0 lds\n"
    "v_mfma_f32_16x16x32_bf16 v[240:243], a[8:11], a[28:31], v[240:243]\n"
    "ds_read_b128 a[40:43], v3 offset:61440\n"
    "v_mfma_f32_16x16x32_bf16 v[148:151], a[0:3], a[16:19], v[148:151]\n"
    "s_xor_b32 s74, s26, 9216\n"
    "ds_read_b64_tr_b16 a[8:9], v7 offset:4096\n"
    "v_mfma_f32_16x16x32_bf16 v[180:183], a[0:3], a[20:23], v[180:183]\n"
    "ds_read_b64_tr_b16 a[10:11], v7 offset:4608\n"
    "v_mfma_f32_16x16x32_bf16 v[212:215], a[0:3], a[24:27], v[212:215]\n"
    "buffer_load_dwordx4 v65, s[44:47], s74 offen offset:1024 lds\n"
    "v_mfma_f32_16x16x32_bf16 v[244:247], a[0:3], a[28:31], v[244:247]\n"
    "s_waitcnt lgkmcnt(3)\n"
    "ds_read_b128 a[44:47], v3 offset:63488\n"
    "v_mfma_f32_16x16x32_bf16 v[152:155], a[4:7], a[16:19], v[152:155]\n"
    "s_waitcnt vmcnt(6)\n"
    "s_xor_b32 s74, s26, 10240\n"
    "ds_read_b64_tr_b16 a[0:1], v4 offset:8192\n"
    "v_mfma_f32_16x16x32_bf16 v[184:187], a[4:7], a[20:23], v[184:187]\n"
    "ds_read_b64_tr_b16 a[2:3], v4 offset:8704\n"
    "v_mfma_f32_16x16x32_bf16 v[216:219], a[4:7], a[24:27], v[216:219]\n"
    "buffer_load_dwordx4 v64, s[44:47], s74 offen offset:2048 lds\n"
    "v_mfma_f32_16x16x32_bf16 v[248:251], a[4:7], a[28:31], v[248:251]\n"
    "s_waitcnt lgkmcnt(3)\n"
    "v_mfma_f32_16x16x32_bf16 v[156:159], a[8:11], a[16:19], v[156:159]\n"
    "s_xor_b32 s74, s26, 11264\n"
    "ds_read_b64_tr_b16 a[4:5], v5 offset:8192\n"
    "v_mfma_f32_16x16x32_bf16 v[188:191], a[8:11], a[20:23], v[188:191]\n"
    "ds_read_b64_tr_b16 a[6:7], v5 offset:8704\n"
    "v_mfma_f32_16x16x32_bf16 v[220:223], a[8:11], a[24:27], v[220:223]\n"
    "buffer_load_dwordx4 v65, s[44:47], s74 offen offset:3072 lds\n"
    "v_mfma_f32_16x16x32_bf16 v[252:255], a[8:11], a[28:31], v[252:255]\n"
    "s_waitcnt lgkmcnt(2)\n"
    "v_mfma_f32_16x16x32_bf16 v[128:131], a[0:3], a[32:35], v[128:131]\n"
    "s_add_u32 m0, s66, 20480\n"
    "s_xor_b32 s74, s26, 8320\n"
    "ds_read_b64_tr_b16 a[8:9], v6 offset:8192\n"
    "v_mfma_f32_16x16x32_bf16 v[160:163], a[0:3], a[36:39], v[160:163]\n"
    "ds_read_b64_tr_b16 a[10:11], v6 offset:8704\n"
    "v_mfma_f32_16x16x32_bf16 v[192:195], a[0:3], a[40:43], v[192:195]\n"
    "buffer_load_dwordx4 v64, s[44:47], s74 offen offset:0 lds\n"
    "v_mfma_f32_16x16x32_bf16 v[224:227], a[0:3], a[44:47], v[224:227]\n"
    "s_waitcnt lgkmcnt(2)\n"
    "v_mfma_f32_16x16x32_bf16 v[132:135], a[4:7], a[32:35], v[132:135]\n"
    "s_xor_b32 s74, s26, 9344\n"
    "ds_read_b64_tr_b16 a[0:1], v7 offset:8192\n"
    "v_mfma_f32_16x16x32_bf16 v[164:167], a[4:7], a[36:39], v[164:167]\n"
    "ds_read_b64_tr_b16 a[2:3], v7 offset:8704\n"
    "v_mfma_f32_16x16x32_bf16 v[196:199], a[4:7], a[40:43], v[196:199]\n"
    "buffer_load_dwordx4 v65, s[44:47], s74 offen offset:1024 lds\n"
    "v_mfma_f32_16x16x32_bf16 v[228:231], a[4:7], a[44:47], v[228:231]\n"
    "s_waitcnt lgkmcnt(2)\n"
    "v_mfma_f32_16x16x32_bf16 v[136:139], a[8:11], a[32:35], v[136:139]\n"
    "s_waitcnt vmcnt(6)\n"
    "s_xor_b32 s74, s26, 10368\n"
    "ds_read_b64_tr_b16 a[64:65], v4 offset:12288\n"
    "v_mfma_f32_16x16x32_bf16 v[168:171], a[8:11], a[36:39], v[168:171]\n"
    "ds_read_b64_tr_b16 a[66:67], v4 offset:12800\n"
    "v_mfma_f32_16x16x32_bf16 v[200:203], a[8:11], a[40:43], v[200:203]\n"
    "buffer_load_dwordx4 v64, s[44:47], s74 offen offset:2048 lds\n"
    "v_mfma_f32_16x16x32_bf16 v[232:235], a[8:11], a[44:47], v[232:235]\n"
    "s_waitcnt lgkmcnt(2)\n"
    "v_mfma_f32_16x16x32_bf16 v[140:143], a[0:3], a[32:35], v[140:143]\n"
    "s_xor_b32 s74, s26, 11392\n"
    "ds_read_b64_tr_b16 a[68:69], v5 offset:12288\n"
    "v_mfma_f32_16x16x32_bf16 v[172:175], a[0:3], a[36:39], v[172:175]\n"
    "ds_read_b64_tr_b16 a[70:71], v5 offset:12800\n"
    "v_mfma_f32_16x16x32_bf16 v[204:207], a[0:3], a[40:43], v[204:207]\n"
    "buffer_load_dwordx4 v65, s[44:47], s74 offen offset:3072 lds\n"
    "v_mfma_f32_16x16x32_bf16 v[236:239], a[0:3], a[44:47], v[236:239]\n"
    "s_waitcnt lgkmcnt(2)\n"
    "ds_read_b64_tr_b16 a[72:73], v6 offset:12288\n"
    "ds_read_b64_tr_b16 a[74:75], v6 offset:12800\n"
    "v_mfma_f32_16x16x32_bf16 v[144:147], a[64:67], a[32:35], v[144:147]\n"
    "s_waitcnt lgkmcnt(2)\n"
    "ds_read_b64_tr_b16 a[76:77], v7 offset:12288\n"
    "v_cvt_pk_bf16_f32 v128, v128, v129\n"
    "v_cvt_pk_bf16_f32 v129, v130, v131\n"
    "v_mfma_f32_16x16x32_bf16 v[148:151], a[68:71], a[32:35], v[148:151]\n"
    "ds_read_b64_tr_b16 a[78:79], v7 offset:12800\n"
    "v_cvt_pk_bf16_f32 v130, v132, v133\n"
    "v_cvt_pk_bf16_f32 v131, v134, v135\n"
    "v_mfma_f32_16x16x32_bf16 v[176:179], a[64:67], a[36:39], v[176:179]\n"
    "s_barrier\n"
    "v_cvt_pk_bf16_f32 v132, v136, v137\n"
    "v_cvt_pk_bf16_f32 v133, v138, v139\n"
    "v_mfma_f32_16x16x32_bf16 v[180:183], a[68:71], a[36:39], v[180:183]\n"
    "v_cvt_pk_bf16_f32 v134, v140, v141\n"
    "v_cvt_pk_bf16_f32 v135, v142, v143\n"
    "v_cvt_pk_bf16_f32 v160, v160, v161\n"
    "v_cvt_pk_bf16_f32 v161, v162, v163\n"
    "v_cvt_pk_bf16_f32 v162, v164, v165\n"
    "v_cvt_pk_bf16_f32 v163, v166, v167\n"
    "v_cvt_pk_bf16_f32 v164, v168, v169\n"
    "v_cvt_pk_bf16_f32 v165, v170, v171\n"
    "v_cvt_pk_bf16_f32 v166, v172, v173\n"
    "v_cvt_pk_bf16_f32 v167, v174, v175\n"
    "v_mfma_f32_16x16x32_bf16 v[208:211], a[64:67], a[40:43], v[208:211]\n"
    "s_waitcnt lgkmcnt(2)\n"
    "v_cvt_pk_bf16_f32 v136, v144, v145\n"
    "v_cvt_pk_bf16_f32 v137, v146, v147\n"
    "v_mfma_f32_16x16x32_bf16 v[152:155], a[72:75], a[32:35], v[152:155]\n"
    "v_cvt_pk_bf16_f32 v138, v148, v149\n"
    "v_cvt_pk_bf16_f32 v139, v150, v151\n"
    "v_mfma_f32_16x16x32_bf16 v[184:187], a[72:75], a[36:39], v[184:187]\n"
    "s_waitcnt lgkmcnt(0)\n"
    "v_cvt_pk_bf16_f32 v192, v192, v193\n"
    "v_cvt_pk_bf16_f32 v193, v194, v195\n"
    "v_mfma_f32_16x16x32_bf16 v[156:159], a[76:79], a[32:35], v[156:159]\n"
    "v_cvt_pk_bf16_f32 v194, v196, v197\n"
    "v_cvt_pk_bf16_f32 v140, v152, v153\n"
    "v_mfma_f32_16x16x32_bf16 v[188:191], a[76:79], a[36:39], v[188:191]\n"
    "v_cvt_pk_bf16_f32 v141, v154, v155\n"
    "v_cvt_pk_bf16_f32 v195, v198, v199\n"
    "v_mfma_f32_16x16x32_bf16 v[212:215], a[68:71], a[40:43], v[212:215]\n"
    "v_cvt_pk_bf16_f32 v196, v200, v201\n"
    "s_nop 0\n"
    "v_cvt_pk_bf16_f32 v142, v156, v157\n"
    "v_mfma_f32_16x16x32_bf16 v[216:219], a[72:75], a[40:43], v[216:219]\n"
    "v_cvt_pk_bf16_f32 v143, v158, v159\n"
    "v_cvt_pk_bf16_f32 v197, v202, v203\n"
    "v_mfma_f32_16x16x32_bf16 v[220:223], a[76:79], a[40:43], v[220:223]\n"
    "v_cvt_pk_bf16_f32 v198, v204, v205\n"
    "v_cvt_pk_bf16_f32 v199, v206, v207\n"
    "v_mfma_f32_16x16x32_bf16 v[240:243], a[64:67], a[44:47], v[240:243]\n"
    "v_cvt_pk_bf16_f32 v168, v176, v177\n"
    "v_cvt_pk_bf16_f32 v169, v178, v179\n"
    "v_mfma_f32_16x16x32_bf16 v[244:247], a[68:71], a[44:47], v[244:247]\n"
    "v_cvt_pk_bf16_f32 v170, v180, v181\n"
    "v_cvt_pk_bf16_f32 v171, v182, v183\n"
    "v_mfma_f32_16x16x32_bf16 v[248:251], a[72:75], a[44:47], v[248:251]\n"
    "v_cvt_pk_bf16_f32 v172, v184, v185\n"
    "v_cvt_pk_bf16_f32 v173, v186, v187\n"
    "v_mfma_f32_16x16x32_bf16 v[252:255], a[76:79], a[44:47], v[252:255]\n"
    "v_cvt_pk_bf16_f32 v174, v188, v189\n"
    "v_cvt_pk_bf16_f32 v175, v190, v191\n"
    "v_mfma_f32_16x16x32_bf16 v[144:147], a[128:131], v[128:131], 0\n"
    "v_cvt_pk_bf16_f32 v224, v224, v225\n"
    "v_cvt_pk_bf16_f32 v225, v226, v227\n"
    "v_mfma_f32_16x16x32_bf16 v[144:147], a[132:135], v[132:135], v[144:147]\n"
    "v_cvt_pk_bf16_f32 v226, v228, v229\n"
    "v_cvt_pk_bf16_f32 v227, v230, v231\n"
    "v_mfma_f32_16x16x32_bf16 v[144:147], a[136:139], v[136:139], v[144:147]\n"
    "v_cvt_pk_bf16_f32 v228, v232, v233\n"
    "v_cvt_pk_bf16_f32 v229, v234, v235\n"
    "v_mfma_f32_16x16x32_bf16 v[144:147], a[140:143], v[140:143], v[144:147]\n"
    "v_cvt_pk_bf16_f32 v230, v236, v237\n"
    "v_cvt_pk_bf16_f32 v231, v238, v239\n"
    "v_mfma_f32_16x16x32_bf16 v[148:151], a[144:147], v[128:131], 0\n"
    "v_cvt_pk_bf16_f32 v200, v208, v209\n"
    "v_cvt_pk_bf16_f32 v201, v210, v211\n"
    "v_mfma_f32_16x16x32_bf16 v[148:151], a[148:151], v[132:135], v[148:151]\n"
    "v_cvt_pk_bf16_f32 v202, v212, v213\n"
    "v_cvt_pk_bf16_f32 v203, v214, v215\n"
    "v_mfma_f32_16x16x32_bf16 v[148:151], a[152:155], v[136:139], v[148:151]\n"
    "v_cvt_pk_bf16_f32 v204, v216, v217\n"
    "v_cvt_pk_bf16_f32 v205, v218, v219\n"
    "v_mfma_f32_16x16x32_bf16 v[148:151], a[156:159], v[140:143], v[148:151]\n"
    "v_cvt_pk_bf16_f32 v206, v220, v221\n"
    "v_cvt_pk_bf16_f32 v207, v222, v223\n"
    "v_mfma_f32_16x16x32_bf16 v[152:155], a[160:163], v[128:131], 0\n"
    "v_cvt_pk_bf16_f32 v232, v240, v241\n"
    "v_cvt_pk_bf16_f32 v233, v242, v243\n"
    "v_mfma_f32_16x16x32_bf16 v[152:155], a[164:167], v[132:135], v[152:155]\n"
    "v_cvt_pk_bf16_f32 v234, v244, v245\n"
    "v_cvt_pk_bf16_f32 v235, v246, v247\n"
    "v_mfma_f32_16x16x32_bf16 v[152:155], a[168:171], v[136:139], v[152:155]\n"
    "v_cvt_pk_bf16_f32 v236, v248, v249\n"
    "v_cvt_pk_bf16_f32 v237, v250, v251\n"
    "v_mfma_f32_16x16x32_bf16 v[152:155], a[172:175], v[140:143], v[152:155]\n"
    "v_cvt_pk_bf16_f32 v238, v252, v253\n"
    "v_cvt_pk_bf16_f32 v239, v254, v255\n"
    "v_mfma_f32_16x16x32_bf16 v[156:159], a[176:179], v[128:131], 0\n"
    "v_max3_f32 v128, v144, v145, v146\n"
    "v_max3_f32 v129, v147, v148, v149\n"
    "v_max3_f32 v128, v128, v150, v151\n"
    "v_mfma_f32_16x16x32_bf16 v[156:159], a[180:183], v[132:135], v[156:159]\n"
    "s_nop 0\n"
    "v_max3_f32 v129, v129, v152, v153\n"
    "v_max3_f32 v128, v128, v154, v155\n"
    "v_mfma_f32_16x16x32_bf16 v[156:159], a[184:187], v[136:139], v[156:159]\n"
    "s_add_u32 s72, s27, 1\n"
    "s_and_b32 s72, s72, 7\n"
    "v_mfma_f32_16x16x32_bf16 v[156:159], a[188:191], v[140:143], v[156:159]\n"
    "s_lshl_b32 s72, s72, 7\n"
    "s_add_u32 s72, s72, s28\n"
    "v_mfma_f32_16x16x32_bf16 v[176:179], a[128:131], v[160:163], 0\n"
    "s_add_u32 s73, s72, 32768\n"
    "s_add_u32 m0, s66, 0\n"
    "v_mfma_f32_16x16x32_bf16 v[176:179], a[132:135], v[164:167], v[176:179]\n"
    "s_nop 1\n"
    "v_max3_f32 v129, v129, v156, v157\n"
    "v_max3_f32 v128, v128, v158, v159\n"
    "v_mfma_f32_16x16x32_bf16 v[176:179], a[136:139], v[168:171], v[176:179]\n"
    "v_max_f32 v128, v128, v129\n"
    "v_mov_b32 v129, v128\n"
    "v_mfma_f32_16x16x32_bf16 v[176:179], a[140:143], v[172:175], v[176:179]\n"
    "s_nop 0\n"
    "v_permlane16_swap_b32 v128, v129\n"
    "v_mfma_f32_16x16x32_bf16 v[180:183], a[144:147], v[160:163], 0\n"
    "v_max_f32 v128, v128, v129\n"
    "v_mov_b32 v129, v128\n"
    "v_mfma_f32_16x16x32_bf16 v[180:183], a[148:151], v[164:167], v[180:183]\n"
    "s_nop 0\n"
    "v_permlane32_swap_b32 v128, v129\n"
    "v_mfma_f32_16x16x32_bf16 v[180:183], a[152:155], v[168:171], v[180:183]\n"
    "v_max_f32 v128, v128, v129\n"
    "v_mul_f32 v128, s78, v128\n"
    "v_mfma_f32_16x16x32_bf16 v[180:183], a[156:159], v[172:175], v[180:183]\n"
    "v_fma_f32 v144, v144, s78, -v128\n"
    "v_fma_f32 v145, v145, s78, -v128\n"
    "v_exp_f32 v144, v144\n"
    "v_exp_f32 v145, v145\n"
    "v_fma_f32 v146, v146, s78, -v128\n"
    "v_fma_f32 v147, v147, s78, -v128\n"
    "v_exp_f32 v146, v146\n"
    "v_exp_f32 v147, v147\n"
    "v_cvt_pk_bf16_f32 v32, v144, v145\n"
    "v_fma_f32 v148, v148, s78, -v128\n"
    "v_fma_f32 v149, v149, s78, -v128\n"
    "v_exp_f32 v148, v148\n"
    "v_exp_f32 v149, v149\n"
    "v_mfma_f32_16x16x32_bf16 v[184:187], a[160:163], v[160:163], 0\n"
    "v_cvt_pk_bf16_f32 v33, v146, v147\n"
    "v_fma_f32 v150, v150, s78, -v128\n"
    "v_mfma_f32_16x16x32_bf16 v[184:187], a[164:167], v[164:167], v[184:187]\n"
    "v_fma_f32 v151, v151, s78, -v128\n"
    "v_exp_f32 v150, v150\n"
    "v_mfma_f32_16x16x32_bf16 v[184:187], a[168:171], v[168:171], v[184:187]\n"
    "v_exp_f32 v151, v151\n"
    "v_mfma_f32_16x16x32_bf16 v[184:187], a[172:175], v[172:175], v[184:187]\n"
    "v_cvt_pk_bf16_f32 v34, v148, v149\n"
    "v_fma_f32 v152, v152, s78, -v128\n"
    "v_mfma_f32_16x16x32_bf16 v[188:191], a[176:179], v[160:163], 0\n"
    "v_fma_f32 v153, v153, s78, -v128\n"
    "v_max3_f32 v160, v176, v177, v178\n"
    "v_mfma_f32_16x16x32_bf16 v[188:191], a[180:183], v[164:167], v[188:191]\n"
    "v_exp_f32 v152, v152\n"
    "v_max3_f32 v161, v179, v180, v181\n"
    "v_mfma_f32_16x16x32_bf16 v[188:191], a[184:187], v[168:171], v[188:191]\n"
    "v_exp_f32 v153, v153\n"
    "v_max3_f32 v160, v160, v182, v183\n"
    "v_cvt_pk_bf16_f32 v35, v150, v151\n"
    "v_mfma_f32_16x16x32_bf16 v[188:191], a[188:191], v[172:175], v[188:191]\n"
    "v_fma_f32 v154, v154, s78, -v128\n"
    "v_max3_f32 v161, v161, v184, v185\n"
    "v_fma_f32 v155, v155, s78, -v128\n"
    "v_max3_f32 v160, v160, v186, v187\n"
    "v_exp_f32 v154, v154\n"
    "v_exp_f32 v155, v155\n"
    "v_cvt_pk_bf16_f32 v36, v152, v153\n"
    "s_nop 0\n"
    "v_max3_f32 v161, v161, v188, v189\n"
    "v_max3_f32 v160, v160, v190, v191\n"
    "v_fma_f32 v156, v156, s78, -v128\n"
    "v_max_f32 v160, v160, v161\n"
    "v_fma_f32 v157, v157, s78, -v128\n"
    "v_mov_b32 v161, v160\n"
    "v_exp_f32 v156, v156\n"
    "v_exp_f32 v157, v157\n"
    "v_permlane16_swap_b32 v160, v161\n"
    "v_cvt_pk_bf16_f32 v37, v154, v155\n"
    "v_max_f32 v160, v160, v161\n"
    "v_fma_f32 v158, v158, s78, -v128\n"
    "v_mov_b32 v161, v160\n"
    "v_fma_f32 v159, v159, s78, -v128\n"
    "v_exp_f32 v158, v158\n"
    "v_permlane32_swap_b32 v160, v161\n"
    "v_exp_f32 v159, v159\n"
    "v_max_f32 v160, v160, v161\n"
    "v_cvt_pk_bf16_f32 v38, v156, v157\n"
    "v_mul_f32 v160, s78, v160\n"
    "v_cvt_pk_bf16_f32 v39, v158, v159\n"
    "v_mfma_f32_16x16x32_bf16 v[208:211], a[128:131], v[192:195], 0\n"
    "v_fma_f32 v176, v176, s78, -v160\n"
    "v_fma_f32 v177, v177, s78, -v160\n"
    "v_mfma_f32_16x16x32_bf16 v[208:211], a[132:135], v[196:199], v[208:211]\n"
    "v_exp_f32 v176, v176\n"
    "v_exp_f32 v177, v177\n"
    "v_mfma_f32_16x16x32_bf16 v[208:211], a[136:139], v[200:203], v[208:211]\n"
    "v_fma_f32 v178, v178, s78, -v160\n"
    "v_fma_f32 v179, v179, s78, -v160\n"
    "v_mfma_f32_16x16x32_bf16 v[208:211], a[140:143], v[204:207], v[208:211]\n"
    "v_exp_f32 v178, v178\n"
    "v_mfma_f32_16x16x32_bf16 v[212:215], a[144:147], v[192:195], 0\n"
    "v_exp_f32 v179, v179\n"
    "v_mfma_f32_16x16x32_bf16 v[212:215], a[148:151], v[196:199], v[212:215]\n"
    "v_cvt_pk_bf16_f32 v40, v176, v177\n"
    "v_fma_f32 v180, v180, s78, -v160\n"
    "v_mfma_f32_16x16x32_bf16 v[212:215], a[152:155], v[200:203], v[212:215]\n"
    "v_fma_f32 v181, v181, s78, -v160\n"
    "v_exp_f32 v180, v180\n"
    "v_mfma_f32_16x16x32_bf16 v[212:215], a[156:159], v[204:207], v[212:215]\n"
    "buffer_load_dwordx4 v12, s[48:51], s72 offen offset:0 lds\n"
    "v_exp_f32 v181, v181\n"
    "v_mfma_f32_16x16x32_bf16 v[216:219], a[160:163], v[192:195], 0\n"
    "v_cvt_pk_bf16_f32 v41, v178, v179\n"
    "v_fma_f32 v182, v182, s78, -v160\n"
    "v_mfma_f32_16x16x32_bf16 v[216:219], a[164:167], v[196:199], v[216:219]\n"
    "v_fma_f32 v183, v183, s78, -v160\n"
    "v_exp_f32 v182, v182\n"
    "v_mfma_f32_16x16x32_bf16 v[216:219], a[168:171], v[200:203], v[216:219]\n"
    "v_exp_f32 v183, v183\n"
    "v_mfma_f32_16x16x32_bf16 v[216:219], a[172:175], v[204:207], v[216:219]\n"
    "buffer_load_dwordx4 v13, s[48:51], s72 offen offset:1024 lds\n"
    "v_cvt_pk_bf16_f32 v42, v180, v181\n"
    "v_mfma_f32_16x16x32_bf16 v[220:223], a[176:179], v[192:195], 0\n"
    "v_fma_f32 v184, v184, s78, -v160\n"
    "v_fma_f32 v185, v185, s78, -v160\n"
    "v_max3_f32 v192, v208, v209, v210\n"
    "v_mfma_f32_16x16x32_bf16 v[220:223], a[180:183], v[196:199], v[220:223]\n"
    "v_exp_f32 v184, v184\n"
    "v_max3_f32 v193, v211, v212, v213\n"
    "v_mfma_f32_16x16x32_bf16 v[220:223], a[184:187], v[200:203], v[220:223]\n"
    "v_exp_f32 v185, v185\n"
    "v_max3_f32 v192, v192, v214, v215\n"
    "v_cvt_pk_bf16_f32 v43, v182, v183\n"
    "v_mfma_f32_16x16x32_bf16 v[220:223], a[188:191], v[204:207], v[220:223]\n"
    "buffer_load_dwordx4 v14, s[48:51], s72 offen offset:2048 lds\n"
    "v_fma_f32 v186, v186, s78, -v160\n"
    "v_max3_f32 v193, v193, v216, v217\n"
    "v_fma_f32 v187, v187, s78, -v160\n"
    "v_max3_f32 v192, v192, v218, v219\n"
    "v_exp_f32 v186, v186\n"
    "v_exp_f32 v187, v187\n"
    "s_nop 0\n"
    "v_max3_f32 v193, v193, v220, v221\n"
    "v_cvt_pk_bf16_f32 v44, v184, v185\n"
    "v_max3_f32 v192, v192, v222, v223\n"
    "v_fma_f32 v188, v188, s78, -v160\n"
    "v_max_f32 v192, v192, v193\n"
    "v_fma_f32 v189, v189, s78, -v160\n"
    "v_mov_b32 v193, v192\n"
    "v_exp_f32 v188, v188\n"
    "v_exp_f32 v189, v189\n"
    "v_permlane16_swap_b32 v192, v193\n"
    "v_cvt_pk_bf16_f32 v45, v186, v187\n"
    "v_max_f32 v192, v192, v193\n"
    "v_fma_f32 v190, v190, s78, -v160\n"
    "v_mov_b32 v193, v192\n"
    "v_fma_f32 v191, v191, s78, -v160\n"
    "v_exp_f32 v190, v190\n"
    "v_permlane32_swap_b32 v192, v193\n"
    "v_exp_f32 v191, v191\n"
    "v_max_f32 v192, v192, v193\n"
    "v_cvt_pk_bf16_f32 v46, v188, v189\n"
    "v_mul_f32 v192, s78, v192\n"
    "v_cvt_pk_bf16_f32 v47, v190, v191\n"
    "buffer_load_dwordx4 v15, s[48:51], s72 offen offset:3072 lds\n"
    "s_add_u32 m0, s66, 4096\n"
    "v_mfma_f32_16x16x32_bf16 v[240:243], a[128:131], v[224:227], 0\n"
    "v_fma_f32 v208, v208, s78, -v192\n"
    "v_fma_f32 v209, v209, s78, -v192\n"
    "v_mfma_f32_16x16x32_bf16 v[240:243], a[132:135], v[228:231], v[240:243]\n"
    "v_exp_f32 v208, v208\n"
    "v_exp_f32 v209, v209\n"
    "v_mfma_f32_16x16x32_bf16 v[240:243], a[136:139], v[232:235], v[240:243]\n"
    "v_fma_f32 v210, v210, s78, -v192\n"
    "v_fma_f32 v211, v211, s78, -v192\n"
    "buffer_load_dwordx4 v12, s[48:51], s73 offen offset:0 lds\n"
    "v_mfma_f32_16x16x32_bf16 v[240:243], a[140:143], v[236:239], v[240:243]\n"
    "v_exp_f32 v210, v210\n"
    "v_mfma_f32_16x16x32_bf16 v[244:247], a[144:147], v[224:227], 0\n"
    "v_exp_f32 v211, v211\n"
    "v_mfma_f32_16x16x32_bf16 v[244:247], a[148:151], v[228:231], v[244:247]\n"
    "v_cvt_pk_bf16_f32 v48, v208, v209\n"
    "v_fma_f32 v212, v212, s78, -v192\n"
    "v_mfma_f32_16x16x32_bf16 v[244:247], a[152:155], v[232:235], v[244:247]\n"
    "v_fma_f32 v213, v213, s78, -v192\n"
    "buffer_load_dwordx4 v13, s[48:51], s73 offen offset:1024 lds\n"
    "v_mfma_f32_16x16x32_bf16 v[244:247], a[156:159], v[236:239], v[244:247]\n"
    "v_exp_f32 v212, v212\n"
    "v_exp_f32 v213, v213\n"
    "v_mfma_f32_16x16x32_bf16 v[248:251], a[160:163], v[224:227], 0\n"
    "v_cvt_pk_bf16_f32 v49, v210, v211\n"
    "v_fma_f32 v214, v214, s78, -v192\n"
    "v_mfma_f32_16x16x32_bf16 v[248:251], a[164:167], v[228:231], v[248:251]\n"
    "v_fma_f32 v215, v215, s78, -v192\n"
    "v_exp_f32 v214, v214\n"
    "buffer_load_dwordx4 v14, s[48:51], s73 offen offset:2048 lds\n"
    "v_mfma_f32_16x16x32_bf16 v[248:251], a[168:171], v[232:235], v[248:251]\n"
    "v_exp_f32 v215, v215\n"
    "v_mfma_f32_16x16x32_bf16 v[248:251], a[172:175], v[236:239], v[248:251]\n"
    "v_cvt_pk_bf16_f32 v50, v212, v213\n"
    "v_fma_f32 v216, v216, s78, -v192\n"
    "v_mfma_f32_16x16x32_bf16 v[252:255], a[176:179], v[224:227], 0\n"
    "v_fma_f32 v217, v217, s78, -v192\n"
    "v_max3_f32 v224, v240, v241, v242\n"
    "v_mfma_f32_16x16x32_bf16 v[252:255], a[180:183], v[228:231], v[252:255]\n"
    "v_exp_f32 v216, v216\n"
    "buffer_load_dwordx4 v15, s[48:51], s73 offen offset:3072 lds\n"
    "v_max3_f32 v225, v243, v244, v245\n"
    "v_mfma_f32_16x16x32_bf16 v[252:255], a[184:187], v[232:235], v[252:255]\n"
    "v_exp_f32 v217, v217\n"
    "v_max3_f32 v224, v224, v246, v247\n"
    "v_cvt_pk_bf16_f32 v51, v214, v215\n"
    "v_mfma_f32_16x16x32_bf16 v[252:255], a[188:191], v[236:239], v[252:255]\n"
    "s_waitcnt vmcnt(8)\n"
    "ds_read_b64_tr_b16 a[224:225], v4 offset:16384\n"
    "v_fma_f32 v218, v218, s78, -v192\n"
    "v_max3_f32 v225, v225, v248, v249\n"
    "v_fma_f32 v219, v219, s78, -v192\n"
    "v_max3_f32 v224, v224, v250, v251\n"
    "v_exp_f32 v218, v218\n"
    "v_exp_f32 v219, v219\n"
    "v_max3_f32 v225, v225, v252, v253\n"
    "v_cvt_pk_bf16_f32 v52, v216, v217\n"
    "v_max3_f32 v224, v224, v254, v255\n"
    "v_fma_f32 v220, v220, s78, -v192\n"
    "v_max_f32 v224, v224, v225\n"
    "v_fma_f32 v221, v221, s78, -v192\n"
    "v_mov_b32 v225, v224\n"
    "v_exp_f32 v220, v220\n"
    "v_exp_f32 v221, v221\n"
    "v_permlane16_swap_b32 v224, v225\n"
    "v_cvt_pk_bf16_f32 v53, v218, v219\n"
    "v_max_f32 v224, v224, v225\n"
    "v_fma_f32 v222, v222, s78, -v192\n"
    "v_mov_b32 v225, v224\n"
    "v_fma_f32 v223, v223, s78, -v192\n"
    "v_exp_f32 v222, v222\n"
    "v_permlane32_swap_b32 v224, v225\n"
    "v_exp_f32 v223, v223\n"
    "v_max_f32 v224, v224, v225\n"
    "v_cvt_pk_bf16_f32 v54, v220, v221\n"
    "v_mul_f32 v224, s78, v224\n"
    "v_cvt_pk_bf16_f32 v55, v222, v223\n"
    "v_mfma_f32_16x16x32_bf16 v[96:99], a[80:83], v[32:35], 0\n"
    "ds_read_b64_tr_b16 a[226:227], v4 offset:16896\n"
    "v_fma_f32 v240, v240, s78, -v224\n"
    "v_fma_f32 v241, v241, s78, -v224\n"
    "v_exp_f32 v240, v240\n"
    "v_mfma_f32_16x16x32_bf16 v[96:99], a[80:83], v[36:39], v[96:99]\n"
    "ds_read_b64_tr_b16 a[228:229], v5 offset:16384\n"
    "v_exp_f32 v241, v241\n"
    "v_fma_f32 v242, v242, s78, -v224\n"
    "v_mfma_f32_16x16x32_bf16 v[128:131], a[192:195], v[32:35], 0\n"
    "ds_read_b64_tr_b16 a[230:231], v5 offset:16896\n"
    "v_fma_f32 v243, v243, s78, -v224\n"
    "v_exp_f32 v242, v242\n"
    "v_exp_f32 v243, v243\n"
    "v_cvt_pk_bf16_f32 v56, v240, v241\n"
    "v_mfma_f32_16x16x32_bf16 v[132:135], a[196:199], v[32:35], 0\n"
    "ds_read_b64_tr_b16 a[232:233], v6 offset:16384\n"
    "v_fma_f32 v244, v244, s78, -v224\n"
    "v_fma_f32 v245, v245, s78, -v224\n"
    "v_exp_f32 v244, v244\n"
    "v_exp_f32 v245, v245\n"
    "s_waitcnt lgkmcnt(3)\n"
    "v_mfma_f32_16x16x32_bf16 v[128:131], a[224:227], v[36:39], v[128:131]\n"
    "ds_read_b64_tr_b16 a[234:235], v6 offset:16896\n"
    "v_cvt_pk_bf16_f32 v57, v242, v243\n"
    "v_mfma_f32_16x16x32_bf16 v[136:139], a[200:203], v[32:35], 0\n"
    "ds_read_b64_tr_b16 a[236:237], v7 offset:16384\n"
    "v_fma_f32 v246, v246, s78, -v224\n"
    "v_fma_f32 v247, v247, s78, -v224\n"
    "v_exp_f32 v246, v246\n"
    "v_exp_f32 v247, v247\n"
    "s_waitcnt lgkmcnt(3)\n"
    "v_mfma_f32_16x16x32_bf16 v[132:135], a[228:231], v[36:39], v[132:135]\n"
    "ds_read_b64_tr_b16 a[238:239], v7 offset:16896\n"
    "v_cvt_pk_bf16_f32 v58, v244, v245\n"
    "v_mfma_f32_16x16x32_bf16 v[140:143], a[204:207], v[32:35], 0\n"
    "ds_read_b64_tr_b16 a[240:241], v4 offset:20480\n"
    "v_fma_f32 v248, v248, s78, -v224\n"
    "v_fma_f32 v249, v249, s78, -v224\n"
    "v_exp_f32 v248, v248\n"
    "v_exp_f32 v249, v249\n"
    "v_mfma_f32_16x16x32_bf16 v[144:147], a[208:211], v[32:35], 0\n"
    "s_waitcnt lgkmcnt(3)\n"
    "ds_read_b64_tr_b16 a[242:243], v4 offset:20992\n"
    "v_cvt_pk_bf16_f32 v59, v246, v247\n"
    "v_mfma_f32_16x16x32_bf16 v[136:139], a[232:235], v[36:39], v[136:139]\n"
    "ds_read_b64_tr_b16 a[244:245], v5 offset:20480\n"
    "v_fma_f32 v250, v250, s78, -v224\n"
    "v_fma_f32 v251, v251, s78, -v224\n"
    "v_exp_f32 v250, v250\n"
    "v_mfma_f32_16x16x32_bf16 v[148:151], a[212:215], v[32:35], 0\n"
    "s_waitcnt lgkmcnt(3)\n"
    "ds_read_b64_tr_b16 a[246:247], v5 offset:20992\n"
    "v_exp_f32 v251, v251\n"
    "v_cvt_pk_bf16_f32 v60, v248, v249\n"
    "v_mfma_f32_16x16x32_bf16 v[140:143], a[236:239], v[36:39], v[140:143]\n"
    "ds_read_b64_tr_b16 a[248:249], v6 offset:20480\n"
    "v_rcp_f32 v96, v96\n"
    "v_fma_f32 v252, v252, s78, -v224\n"
    "v_mul_f32 v128, v96, v128\n"
    "v_mul_f32 v129, v96, v129\n"
    "v_fma_f32 v253, v253, s78, -v224\n"
    "v_mul_f32 v130, v96, v130\n"
    "v_exp_f32 v252, v252\n"
    "v_mul_f32 v131, v96, v131\n"
    "s_waitcnt lgkmcnt(3)\n"
    "v_mfma_f32_16x16x32_bf16 v[144:147], a[240:243], v[36:39], v[144:147]\n"
    "ds_read_b64_tr_b16 a[250:251], v6 offset:20992\n"
    "v_mul_f32 v132, v96, v132\n"
    "v_mfma_f32_16x16x32_bf16 v[152:155], a[216:219], v[32:35], 0\n"
    "s_waitcnt lgkmcnt(2)\n"
    "ds_read_b64_tr_b16 a[252:253], v7 offset:20480\n"
    "v_exp_f32 v253, v253\n"
    "v_mul_f32 v133, v96, v133\n"
    "v_cvt_pk_bf16_f32 v61, v250, v251\n"
    "v_mfma_f32_16x16x32_bf16 v[148:151], a[244:247], v[36:39], v[148:151]\n"
    "ds_read_b64_tr_b16 a[254:255], v7 offset:20992\n"
    "v_mul_f32 v134, v96, v134\n"
    "v_fma_f32 v254, v254, s78, -v224\n"
    "v_mul_f32 v135, v96, v135\n"
    "v_cvt_pk_bf16_f32 v128, v128, v129\n"
    "v_fma_f32 v255, v255, s78, -v224\n"
    "v_cvt_pk_bf16_f32 v129, v130, v131\n"
    "v_mfma_f32_16x16x32_bf16 v[156:159], a[220:223], v[32:35], 0\n"
    "v_exp_f32 v254, v254\n"
    "v_cvt_pk_bf16_f32 v130, v132, v133\n"
    "v_exp_f32 v255, v255\n"
    "v_cvt_pk_bf16_f32 v131, v134, v135\n"
    "v_cvt_pk_bf16_f32 v62, v252, v253\n"
    "v_mul_f32 v136, v96, v136\n"
    "s_waitcnt lgkmcnt(2)\n"
    "v_mfma_f32_16x16x32_bf16 v[152:155], a[248:251], v[36:39], v[152:155]\n"
    "ds_write_b128 v20, v[128:131] offset:0\n"
    "v_cvt_pk_bf16_f32 v63, v254, v255\n"
    "v_mul_f32 v137, v96, v137\n"
    "v_mfma_f32_16x16x32_bf16 v[100:103], a[80:83], v[40:43], 0\n"
    "v_mul_f32 v138, v96, v138\n"
    "v_mul_f32 v139, v96, v139\n"
    "v_mul_f32 v140, v96, v140\n"
    "v_mfma_f32_16x16x32_bf16 v[100:103], a[80:83], v[44:47], v[100:103]\n"
    "s_waitcnt lgkmcnt(1)\n"
    "v_mul_f32 v141, v96, v141\n"
    "v_mul_f32 v142, v96, v142\n"
    "v_mfma_f32_16x16x32_bf16 v[156:159], a[252:255], v[36:39], v[156:159]\n"
    "v_mul_f32 v143, v96, v143\n"
    "v_cvt_pk_bf16_f32 v136, v136, v137\n"
    "v_mfma_f32_16x16x32_bf16 v[160:163], a[192:195], v[40:43], 0\n"
    "v_cvt_pk_bf16_f32 v137, v138, v139\n"
    "v_cvt_pk_bf16_f32 v138, v140, v141\n"
    "v_mfma_f32_16x16x32_bf16 v[160:163], a[224:227], v[44:47], v[160:163]\n"
    "v_cvt_pk_bf16_f32 v139, v142, v143\n"
    "v_mul_f32 v144, v96, v144\n"
    "v_mfma_f32_16x16x32_bf16 v[164:167], a[196:199], v[40:43], 0\n"
    "ds_write_b128 v20, v[136:139] offset:4096\n"
    "v_mul_f32 v145, v96, v145\n"
    "v_mul_f32 v146, v96, v146\n"
    "v_mul_f32 v147, v96, v147\n"
    "v_mfma_f32_16x16x32_bf16 v[164:167], a[228:231], v[44:47], v[164:167]\n"
    "v_mul_f32 v148, v96, v148\n"
    "v_mul_f32 v149, v96, v149\n"
    "v_mul_f32 v150, v96, v150\n"
    "v_mfma_f32_16x16x32_bf16 v[168:171], a[200:203], v[40:43], 0\n"
    "v_mul_f32 v151, v96, v151\n"
    "v_cvt_pk_bf16_f32 v144, v144, v145\n"
    "v_cvt_pk_bf16_f32 v145, v146, v147\n"
    "v_mfma_f32_16x16x32_bf16 v[168:171], a[232:235], v[44:47], v[168:171]\n"
    "v_cvt_pk_bf16_f32 v146, v148, v149\n"
    "v_cvt_pk_bf16_f32 v147, v150, v151\n"
    "v_mfma_f32_16x16x32_bf16 v[172:175], a[204:207], v[40:43], 0\n"
    "ds_write_b128 v20, v[144:147] offset:8192\n"
    "v_mul_f32 v152, v96, v152\n"
    "v_mul_f32 v153, v96, v153\n"
    "v_mul_f32 v154, v96, v154\n"
    "v_mfma_f32_16x16x32_bf16 v[172:175], a[236:239], v[44:47], v[172:175]\n"
    "s_xor_b32 s72, s72, 262144\n"
    "v_mul_f32 v155, v96, v155\n"
    "v_mul_f32 v156, v96, v156\n"
    "v_mul_f32 v157, v96, v157\n"
    "v_mfma_f32_16x16x32_bf16 v[176:179], a[208:211], v[40:43], 0\n"
    "v_mul_f32 v158, v96, v158\n"
    "v_mul_f32 v159, v96, v159\n"
    "v_cvt_pk_bf16_f32 v152, v152, v153\n"
    "v_mfma_f32_16x16x32_bf16 v[176:179], a[240:243], v[44:47], v[176:179]\n"
    "v_cvt_pk_bf16_f32 v153, v154, v155\n"
    "v_cvt_pk_bf16_f32 v154, v156, v157\n"
    "v_cvt_pk_bf16_f32 v155, v158, v159\n"
    "v_mfma_f32_16x16x32_bf16 v[180:183], a[212:215], v[40:43], 0\n"
    "s_add_u32 s73, s72, 32768\n"
    "ds_write_b128 v20, v[152:155] offset:12288\n"
    "v_rcp_f32 v100, v100\n"
    "v_mfma_f32_16x16x32_bf16 v[180:183], a[244:247], v[44:47], v[180:183]\n"
    "v_mul_f32 v160, v100, v160\n"
    "v_mul_f32 v161, v100, v161\n"
    "v_mul_f32 v162, v100, v162\n"
    "v_mul_f32 v163, v100, v163\n"
    "v_mfma_f32_16x16x32_bf16 v[184:187], a[216:219], v[40:43], 0\n"
    "v_mul_f32 v164, v100, v164\n"
    "v_mul_f32 v165, v100, v165\n"
    "v_mul_f32 v166, v100, v166\n"
    "v_mfma_f32_16x16x32_bf16 v[184:187], a[248:251], v[44:47], v[184:187]\n"
    "s_add_u32 m0, s66, 8192\n"
    "v_mul_f32 v167, v100, v167\n"
    "v_cvt_pk_bf16_f32 v160, v160, v161\n"
    "v_cvt_pk_bf16_f32 v161, v162, v163\n"
    "v_mfma_f32_16x16x32_bf16 v[188:191], a[220:223], v[40:43], 0\n"
    "v_cvt_pk_bf16_f32 v162, v164, v165\n"
    "v_cvt_pk_bf16_f32 v163, v166, v167\n"
    "v_mfma_f32_16x16x32_bf16 v[188:191], a[252:255], v[44:47], v[188:191]\n"
    "ds_write_b128 v20, v[160:163] offset:1024\n"
    "v_mul_f32 v168, v100, v168\n"
    "v_mul_f32 v169, v100, v169\n"
    "v_mfma_f32_16x16x32_bf16 v[104:107], a[80:83], v[48:51], 0\n"
    "v_mul_f32 v170, v100, v170\n"
    "v_mul_f32 v171, v100, v171\n"
    "v_mul_f32 v172, v100, v172\n"
    "v_mfma_f32_16x16x32_bf16 v[104:107], a[80:83], v[52:55], v[104:107]\n"
    "v_mul_f32 v173, v100, v173\n"
    "v_mul_f32 v174, v100, v174\n"
    "v_mul_f32 v175, v100, v175\n"
    "v_mfma_f32_16x16x32_bf16 v[192:195], a[192:195], v[48:51], 0\n"
    "v_cvt_pk_bf16_f32 v168, v168, v169\n"
    "v_cvt_pk_bf16_f32 v169, v170, v171\n"
    "v_cvt_pk_bf16_f32 v170, v172, v173\n"
    "v_mfma_f32_16x16x32_bf16 v[192:195], a[224:227], v[52:55], v[192:195]\n"
    "v_cvt_pk_bf16_f32 v171, v174, v175\n"
    "v_mul_f32 v176, v100, v176\n"
    "v_mfma_f32_16x16x32_bf16 v[196:199], a[196:199], v[48:51], 0\n"
    "ds_write_b128 v20, v[168:171] offset:5120\n"
    "v_mul_f32 v177, v100, v177\n"
    "v_mul_f32 v178, v100, v178\n"
    "v_mul_f32 v179, v100, v179\n"
    "v_mfma_f32_16x16x32_bf16 v[196:199], a[228:231], v[52:55], v[196:199]\n"
    "v_mul_f32 v180, v100, v180\n"
    "v_mul_f32 v181, v100, v181\n"
    "v_mul_f32 v182, v100, v182\n"
    "v_mfma_f32_16x16x32_bf16 v[200:203], a[200:203], v[48:51], 0\n"
    "v_mul_f32 v183, v100, v183\n"
    "v_cvt_pk_bf16_f32 v176, v176, v177\n"
    "v_cvt_pk_bf16_f32 v177, v178, v179\n"
    "v_mfma_f32_16x16x32_bf16 v[200:203], a[232:235], v[52:55], v[200:203]\n"
    "v_cvt_pk_bf16_f32 v178, v180, v181\n"
    "v_cvt_pk_bf16_f32 v179, v182, v183\n"
    "v_mfma_f32_16x16x32_bf16 v[204:207], a[204:207], v[48:51], 0\n"
    "ds_write_b128 v20, v[176:179] offset:9216\n"
    "v_mul_f32 v184, v100, v184\n"
    "v_mul_f32 v185, v100, v185\n"
    "v_mul_f32 v186, v100, v186\n"
    "v_mfma_f32_16x16x32_bf16 v[204:207], a[236:239], v[52:55], v[204:207]\n"
    "buffer_load_dwordx4 v12, s[48:51], s72 offen offset:0 lds\n"
    "v_mul_f32 v187, v100, v187\n"
    "v_mul_f32 v188, v100, v188\n"
    "v_mul_f32 v189, v100, v189\n"
    "v_mfma_f32_16x16x32_bf16 v[208:211], a[208:211], v[48:51], 0\n"
    "v_mul_f32 v190, v100, v190\n"
    "v_mul_f32 v191, v100, v191\n"
    "v_cvt_pk_bf16_f32 v184, v184, v185\n"
    "v_mfma_f32_16x16x32_bf16 v[208:211], a[240:243], v[52:55], v[208:211]\n"
    "v_cvt_pk_bf16_f32 v185, v186, v187\n"
    "v_cvt_pk_bf16_f32 v186, v188, v189\n"
    "v_cvt_pk_bf16_f32 v187, v190, v191\n"
    "v_mfma_f32_16x16x32_bf16 v[212:215], a[212:215], v[48:51], 0\n"
    "buffer_load_dwordx4 v13, s[48:51], s72 offen offset:1024 lds\n"
    "v_rcp_f32 v104, v104\n"
    "v_mfma_f32_16x16x32_bf16 v[212:215], a[244:247], v[52:55], v[212:215]\n"
    "ds_write_b128 v20, v[184:187] offset:13312\n"
    "v_mul_f32 v192, v104, v192\n"
    "v_mul_f32 v193, v104, v193\n"
    "v_mul_f32 v194, v104, v194\n"
    "v_mul_f32 v195, v104, v195\n"
    "v_mfma_f32_16x16x32_bf16 v[216:219], a[216:219], v[48:51], 0\n"
    "v_mul_f32 v196, v104, v196\n"
    "v_mul_f32 v197, v104, v197\n"
    "v_mul_f32 v198, v104, v198\n"
    "v_mfma_f32_16x16x32_bf16 v[216:219], a[248:251], v[52:55], v[216:219]\n"
    "buffer_load_dwordx4 v14, s[48:51], s72 offen offset:2048 lds\n"
    "v_mul_f32 v199, v104, v199\n"
    "v_cvt_pk_bf16_f32 v192, v192, v193\n"
    "v_cvt_pk_bf16_f32 v193, v194, v195\n"
    "v_mfma_f32_16x16x32_bf16 v[220:223], a[220:223], v[48:51], 0\n"
    "v_cvt_pk_bf16_f32 v194, v196, v197\n"
    "v_cvt_pk_bf16_f32 v195, v198, v199\n"
    "v_mfma_f32_16x16x32_bf16 v[220:223], a[252:255], v[52:55], v[220:223]\n"
    "ds_write_b128 v20, v[192:195] offset:2048\n"
    "v_mul_f32 v200, v104, v200\n"
    "v_mul_f32 v201, v104, v201\n"
    "v_mfma_f32_16x16x32_bf16 v[108:111], a[80:83], v[56:59], 0\n"
    "v_mul_f32 v202, v104, v202\n"
    "v_mul_f32 v203, v104, v203\n"
    "v_mul_f32 v204, v104, v204\n"
    "v_mfma_f32_16x16x32_bf16 v[108:111], a[80:83], v[60:63], v[108:111]\n"
    "buffer_load_dwordx4 v15, s[48:51], s72 offen offset:3072 lds\n"
    "v_mul_f32 v205, v104, v205\n"
    "v_mul_f32 v206, v104, v206\n"
    "v_mul_f32 v207, v104, v207\n"
    "v_mfma_f32_16x16x32_bf16 v[224:227], a[192:195], v[56:59], 0\n"
    "s_add_u32 m0, s66, 12288\n"
    "v_cvt_pk_bf16_f32 v200, v200, v201\n"
    "v_cvt_pk_bf16_f32 v201, v202, v203\n"
    "v_cvt_pk_bf16_f32 v202, v204, v205\n"
    "v_mfma_f32_16x16x32_bf16 v[224:227], a[224:227], v[60:63], v[224:227]\n"
    "v_cvt_pk_bf16_f32 v203, v206, v207\n"
    "v_mul_f32 v208, v104, v208\n"
    "v_mfma_f32_16x16x32_bf16 v[228:231], a[196:199], v[56:59], 0\n"
    "ds_write_b128 v20, v[200:203] offset:6144\n"
    "v_mul_f32 v209, v104, v209\n"
    "v_mul_f32 v210, v104, v210\n"
    "v_mul_f32 v211, v104, v211\n"
    "v_mfma_f32_16x16x32_bf16 v[228:231], a[228:231], v[60:63], v[228:231]\n"
    "buffer_load_dwordx4 v12, s[48:51], s73 offen offset:0 lds\n"
    "v_mul_f32 v212, v104, v212\n"
    "v_mul_f32 v213, v104, v213\n"
    "v_mul_f32 v214, v104, v214\n"
    "v_mfma_f32_16x16x32_bf16 v[232:235], a[200:203], v[56:59], 0\n"
    "v_mul_f32 v215, v104, v215\n"
    "v_cvt_pk_bf16_f32 v208, v208, v209\n"
    "v_cvt_pk_bf16_f32 v209, v210, v211\n"
    "v_mfma_f32_16x16x32_bf16 v[232:235], a[232:235], v[60:63], v[232:235]\n"
    "v_cvt_pk_bf16_f32 v210, v212, v213\n"
    "v_cvt_pk_bf16_f32 v211, v214, v215\n"
    "v_mfma_f32_16x16x32_bf16 v[236:239], a[204:207], v[56:59], 0\n"
    "ds_write_b128 v20, v[208:211] offset:10240\n"
    "v_mul_f32 v216, v104, v216\n"
    "v_mul_f32 v217, v104, v217\n"
    "v_mul_f32 v218, v104, v218\n"
    "v_mfma_f32_16x16x32_bf16 v[236:239], a[236:239], v[60:63], v[236:239]\n"
    "buffer_load_dwordx4 v13, s[48:51], s73 offen offset:1024 lds\n"
    "v_mul_f32 v219, v104, v219\n"
    "v_mul_f32 v220, v104, v220\n"
    "v_mul_f32 v221, v104, v221\n"
    "v_mfma_f32_16x16x32_bf16 v[240:243], a[208:211], v[56:59], 0\n"
    "v_mul_f32 v222, v104, v222\n"
    "v_mul_f32 v223, v104, v223\n"
    "v_cvt_pk_bf16_f32 v216, v216, v217\n"
    "v_mfma_f32_16x16x32_bf16 v[240:243], a[240:243], v[60:63], v[240:243]\n"
    "v_cvt_pk_bf16_f32 v217, v218, v219\n"
    "v_cvt_pk_bf16_f32 v218, v220, v221\n"
    "v_cvt_pk_bf16_f32 v219, v222, v223\n"
    "v_mfma_f32_16x16x32_bf16 v[244:247], a[212:215], v[56:59], 0\n"
    "buffer_load_dwordx4 v14, s[48:51], s73 offen offset:2048 lds\n"
    "v_rcp_f32 v108, v108\n"
    "v_mfma_f32_16x16x32_bf16 v[244:247], a[244:247], v[60:63], v[244:247]\n"
    "ds_write_b128 v20, v[216:219] offset:14336\n"
    "v_mul_f32 v224, v108, v224\n"
    "v_mul_f32 v225, v108, v225\n"
    "v_mul_f32 v226, v108, v226\n"
    "v_mul_f32 v227, v108, v227\n"
    "v_mfma_f32_16x16x32_bf16 v[248:251], a[216:219], v[56:59], 0\n"
    "v_mul_f32 v228, v108, v228\n"
    "v_mul_f32 v229, v108, v229\n"
    "v_mul_f32 v230, v108, v230\n"
    "v_mfma_f32_16x16x32_bf16 v[248:251], a[248:251], v[60:63], v[248:251]\n"
    "buffer_load_dwordx4 v15, s[48:51], s73 offen offset:3072 lds\n"
    "v_mul_f32 v231, v108, v231\n"
    "v_cvt_pk_bf16_f32 v224, v224, v225\n"
    "v_cvt_pk_bf16_f32 v225, v226, v227\n"
    "v_mfma_f32_16x16x32_bf16 v[252:255], a[220:223], v[56:59], 0\n"
    "v_cvt_pk_bf16_f32 v226, v228, v229\n"
    "v_cvt_pk_bf16_f32 v227, v230, v231\n"
    "v_mfma_f32_16x16x32_bf16 v[252:255], a[252:255], v[60:63], v[252:255]\n"
    "ds_write_b128 v20, v[224:227] offset:3072\n"
    "v_mul_f32 v232, v108, v232\n"
    "v_mul_f32 v233, v108, v233\n"
    "v_mul_f32 v234, v108, v234\n"
    "v_mul_f32 v235, v108, v235\n"
    "v_mul_f32 v236, v108, v236\n"
    "v_mul_f32 v237, v108, v237\n"
    "v_mul_f32 v238, v108, v238\n"
    "v_mul_f32 v239, v108, v239\n"
    "v_cvt_pk_bf16_f32 v232, v232, v233\n"
    "v_cvt_pk_bf16_f32 v233, v234, v235\n"
    "v_cvt_pk_bf16_f32 v234, v236, v237\n"
    "v_cvt_pk_bf16_f32 v235, v238, v239\n"
    "v_mul_f32 v240, v108, v240\n"
    "v_mul_f32 v241, v108, v241\n"
    "v_mul_f32 v242, v108, v242\n"
    "v_mul_f32 v243, v108, v243\n"
    "v_mul_f32 v244, v108, v244\n"
    "v_mul_f32 v245, v108, v245\n"
    "v_mul_f32 v246, v108, v246\n"
    "v_mul_f32 v247, v108, v247\n"
    "v_cvt_pk_bf16_f32 v240, v240, v241\n"
    "v_cvt_pk_bf16_f32 v241, v242, v243\n"
    "v_cvt_pk_bf16_f32 v242, v244, v245\n"
    "v_cvt_pk_bf16_f32 v243, v246, v247\n"
    "v_mul_f32 v248, v108, v248\n"
    "v_mul_f32 v249, v108, v249\n"
    "v_mul_f32 v250, v108, v250\n"
    "v_mul_f32 v251, v108, v251\n"
    "v_mul_f32 v252, v108, v252\n"
    "v_mul_f32 v253, v108, v253\n"
    "v_mul_f32 v254, v108, v254\n"
    "v_mul_f32 v255, v108, v255\n"
    "v_cvt_pk_bf16_f32 v248, v248, v249\n"
    "v_cvt_pk_bf16_f32 v249, v250, v251\n"
    "v_cvt_pk_bf16_f32 v250, v252, v253\n"
    "v_cvt_pk_bf16_f32 v251, v254, v255\n"
    "s_add_u32 s72, s27, 2\n"
    "s_and_b32 s72, s72, 7\n"
    "s_lshl_b32 s72, s72, 7\n"
    "s_add_u32 s72, s72, s28\n"
    "s_add_u32 s73, s72, 32768\n"
    "s_add_u32 m0, s66, 16384\n"
    "ds_write_b128 v20, v[232:235] offset:7168\n"
    "buffer_load_dwordx4 v12, s[48:51], s72 offen offset:0 lds\n"
    "ds_write_b128 v20, v[240:243] offset:11264\n"
    "buffer_load_dwordx4 v13, s[48:51], s72 offen offset:1024 lds\n"
    "ds_write_b128 v20, v[248:251] offset:15360\n"
    "buffer_load_dwordx4 v14, s[48:51], s72 offen offset:2048 lds\n"
    "buffer_load_dwordx4 v15, s[48:51], s72 offen offset:3072 lds\n"
    "s_add_u32 m0, s66, 20480\n"
    "s_nop 0\n"
    "buffer_load_dwordx4 v12, s[48:51], s73 offen offset:0 lds\n"
    "buffer_load_dwordx4 v13, s[48:51], s73 offen offset:1024 lds\n"
    "buffer_load_dwordx4 v14, s[48:51], s73 offen offset:2048 lds\n"
    "buffer_load_dwordx4 v15, s[48:51], s73 offen offset:3072 lds\n"
    "s_waitcnt vmcnt(16) lgkmcnt(0)\n"
    "s_barrier\n"
    "ds_read_b64_tr_b16 v[64:65], v17 offset:0\n"
    "ds_read_b64_tr_b16 v[66:67], v17 offset:2048\n"
    "ds_read_b128 a[0:3], v29 offset:0\n"
    "ds_read_b64_tr_b16 v[68:69], v17 offset:4096\n"
    "ds_read_b64_tr_b16 v[70:71], v17 offset:6144\n"
    "ds_read_b128 a[16:19], v29 offset:4096\n"
    "ds_read_b64_tr_b16 v[72:73], v17 offset:24576\n"
    "ds_read_b64_tr_b16 v[74:75], v17 offset:26624\n"
    "ds_read_b128 a[32:35], v29 offset:8192\n"
    "ds_read_b64_tr_b16 v[76:77], v17 offset:28672\n"
    "ds_read_b64_tr_b16 v[78:79], v17 offset:30720\n"
    "ds_read_b128 a[48:51], v29 offset:12288\n"
    "ds_read_b128 a[4:7], v29 offset:1024\n"
    "ds_read_b128 a[20:23], v29 offset:5120\n"
    "ds_read_b128 a[36:39], v29 offset:9216\n"
    "ds_read_b128 a[52:55], v29 offset:13312\n"
    "s_mov_b32 s88, 0\n"
    "s_mov_b32 s30, 0\n"
    "s_mov_b32 s31, 0\n"
    "s_waitcnt lgkmcnt(13)\n"
    "v_mfma_f32_16x16x32_bf16 v[32:35], v[64:67], a[0:3], 0\n"
    "s_waitcnt lgkmcnt(10)\n"
    "ds_read_b64_tr_b16 v[80:81], v18 offset:0\n"
    "ds_read_b128 a[8:11], v29 offset:2048\n"
    "v_mfma_f32_16x16x32_bf16 v[32:35], v[68:71], a[16:19], v[32:35]\n"
    "s_waitcnt lgkmcnt(9)\n"
    "ds_read_b64_tr_b16 v[82:83], v18 offset:2048\n"
    "ds_read_b128 a[24:27], v29 offset:6144\n"
    "v_mfma_f32_16x16x32_bf16 v[32:35], v[72:75], a[32:35], v[32:35]\n"
    "s_waitcnt lgkmcnt(8)\n"
    "ds_read_b128 a[40:43], v29 offset:10240\n"
    "buffer_store_dword v28, v26, s[52:55], 0 offen sc1\n"
    "v_mfma_f32_16x16x32_bf16 v[32:35], v[76:79], a[48:51], v[32:35]\n"
    "s_waitcnt lgkmcnt(8)\n"
    "ds_read_b64_tr_b16 v[84:85], v18 offset:4096\n"
    "ds_read_b128 a[56:59], v29 offset:14336\n"
    "v_mfma_f32_16x16x32_bf16 v[36:39], v[64:67], a[4:7], 0\n"
    "s_waitcnt lgkmcnt(9)\n"
    "ds_read_b64_tr_b16 v[86:87], v18 offset:6144\n"
    "ds_read_b128 a[12:15], v29 offset:3072\n"
    "v_mfma_f32_16x16x32_bf16 v[36:39], v[68:71], a[20:23], v[36:39]\n"
    "s_waitcnt lgkmcnt(10)\n"
    "ds_read_b64_tr_b16 v[88:89], v18 offset:24576\n"
    "ds_read_b128 a[28:31], v29 offset:7168\n"
    "v_mfma_f32_16x16x32_bf16 v[36:39], v[72:75], a[36:39], v[36:39]\n"
    "s_waitcnt lgkmcnt(11)\n"
    "ds_read_b128 a[44:47], v29 offset:11264\n"
    "v_mfma_f32_16x16x32_bf16 v[36:39], v[76:79], a[52:55], v[36:39]\n"
    "s_waitcnt lgkmcnt(10)\n"
    "ds_read_b64_tr_b16 v[90:91], v18 offset:26624\n"
    "ds_read_b128 a[60:63], v29 offset:15360\n"
    "v_mfma_f32_16x16x32_bf16 v[40:43], v[64:67], a[8:11], 0\n"
    "s_waitcnt lgkmcnt(10)\n"
    "ds_read_b64_tr_b16 v[92:93], v18 offset:28672\n"
    "ds_read_b128 a[64:67], v29 offset:16384\n"
    "v_mfma_f32_16x16x32_bf16 v[40:43], v[68:71], a[24:27], v[40:43]\n"
    "s_waitcnt lgkmcnt(11)\n"
    "ds_read_b64_tr_b16 v[94:95], v18 offset:30720\n"
    "ds_read_b128 a[80:83], v29 offset:20480\n"
    "v_mfma_f32_16x16x32_bf16 v[40:43], v[72:75], a[40:43], v[40:43]\n"
    "s_waitcnt lgkmcnt(11)\n"
    "ds_read_b128 a[96:99], v29 offset:24576\n"
    "v_mfma_f32_16x16x32_bf16 v[40:43], v[76:79], a[56:59], v[40:43]\n"
    "s_waitcnt lgkmcnt(10)\n"
    "ds_read_b128 a[112:115], v29 offset:28672\n"
    "v_mfma_f32_16x16x32_bf16 v[44:47], v[64:67], a[12:15], 0\n"
    "s_waitcnt lgkmcnt(9)\n"
    "ds_read_b128 a[68:71], v29 offset:17408\n"
    "ds_read_b128 a[72:75], v29 offset:18432\n"
    "v_mfma_f32_16x16x32_bf16 v[44:47], v[68:71], a[28:31], v[44:47]\n"
    "s_waitcnt lgkmcnt(10)\n"
    "ds_read_b128 a[84:87], v29 offset:21504\n"
    "v_mfma_f32_16x16x32_bf16 v[44:47], v[72:75], a[44:47], v[44:47]\n"
    "s_waitcnt lgkmcnt(9)\n"
    "ds_read_b128 a[100:103], v29 offset:25600\n"
    "v_mfma_f32_16x16x32_bf16 v[44:47], v[76:79], a[60:63], v[44:47]\n"
    "s_waitcnt vmcnt(9) lgkmcnt(7)\n"
    "ds_read_b128 a[116:119], v29 offset:29696\n"
    "s_barrier\n"
    "v_mfma_f32_16x16x32_bf16 v[32:35], v[80:83], a[64:67], v[32:35]\n"
    "s_waitcnt lgkmcnt(7)\n"
    "ds_read_b64_tr_b16 v[64:65], v17 offset:8192\n"
    "ds_read_b128 a[88:91], v29 offset:22528\n"
    "v_mfma_f32_16x16x32_bf16 v[32:35], v[84:87], a[80:83], v[32:35]\n"
    "s_xor_b32 s72, s72, 262144\n"
    "s_add_u32 s73, s72, 32768\n"
    "s_add_u32 m0, s66, 0\n"
    "ds_read_b64_tr_b16 v[66:67], v17 offset:10240\n"
    "ds_read_b128 a[104:107], v29 offset:26624\n"
    "s_waitcnt lgkmcnt(10)\n"
    "v_mfma_f32_16x16x32_bf16 v[32:35], v[88:91], a[96:99], v[32:35]\n"
    "s_waitcnt lgkmcnt(9)\n"
    "buffer_load_dwordx4 v12, s[48:51], s72 offen offset:0 lds\n"
    "v_mfma_f32_16x16x32_bf16 v[32:35], v[92:95], a[112:115], v[32:35]\n"
    "s_waitcnt lgkmcnt(8)\n"
    "ds_read_b64_tr_b16 v[68:69], v17 offset:12288\n"
    "ds_read_b128 a[120:123], v29 offset:30720\n"
    "ds_read_b128 a[76:79], v29 offset:19456\n"
    "v_mfma_f32_16x16x32_bf16 v[36:39], v[80:83], a[68:71], v[36:39]\n"
    "s_waitcnt lgkmcnt(9)\n"
    "ds_read_b64_tr_b16 v[70:71], v17 offset:14336\n"
    "ds_read_b128 a[92:95], v29 offset:23552\n"
    "v_mfma_f32_16x16x32_bf16 v[36:39], v[84:87], a[84:87], v[36:39]\n"
    "s_waitcnt lgkmcnt(10)\n"
    "ds_read_b64_tr_b16 v[72:73], v17 offset:32768\n"
    "ds_read_b128 a[108:111], v29 offset:27648\n"
    "v_mfma_f32_16x16x32_bf16 v[36:39], v[88:91], a[100:103], v[36:39]\n"
    "s_waitcnt lgkmcnt(11)\n"
    "buffer_load_dwordx4 v13, s[48:51], s72 offen offset:1024 lds\n"
    "v_mfma_f32_16x16x32_bf16 v[36:39], v[92:95], a[116:119], v[36:39]\n"
    "ds_read_b64_tr_b16 v[74:75], v17 offset:34816\n"
    "ds_read_b128 a[124:127], v29 offset:31744\n"
    "ds_read_b128 a[128:131], v30 offset:0\n"
    "v_mfma_f32_16x16x32_bf16 v[40:43], v[80:83], a[72:75], v[40:43]\n"
    "s_waitcnt lgkmcnt(12)\n"
    "ds_read_b64_tr_b16 v[76:77], v17 offset:36864\n"
    "ds_read_b128 a[144:147], v30 offset:4096\n"
    "v_mfma_f32_16x16x32_bf16 v[40:43], v[84:87], a[88:91], v[40:43]\n"
    "s_waitcnt lgkmcnt(12)\n"
    "ds_read_b64_tr_b16 v[78:79], v17 offset:38912\n"
    "ds_read_b128 a[160:163], v30 offset:8192\n"
    "v_mfma_f32_16x16x32_bf16 v[40:43], v[88:91], a[104:107], v[40:43]\n"
    "s_waitcnt lgkmcnt(12)\n"
    "buffer_load_dwordx4 v14, s[48:51], s72 offen offset:2048 lds\n"
    "v_mfma_f32_16x16x32_bf16 v[40:43], v[92:95], a[120:123], v[40:43]\n"
    "s_waitcnt lgkmcnt(11)\n"
    "ds_read_b128 a[176:179], v30 offset:12288\n"
    "ds_read_b128 a[132:135], v30 offset:1024\n"
    "v_mfma_f32_16x16x32_bf16 v[44:47], v[80:83], a[76:79], v[44:47]\n"
    "s_waitcnt lgkmcnt(11)\n"
    "ds_read_b128 a[148:151], v30 offset:5120\n"
    "ds_read_b128 a[152:155], v30 offset:6144\n"
    "v_mfma_f32_16x16x32_bf16 v[44:47], v[84:87], a[92:95], v[44:47]\n"
    "s_waitcnt lgkmcnt(11)\n"
    "ds_read_b128 a[164:167], v30 offset:9216\n"
    "v_mfma_f32_16x16x32_bf16 v[44:47], v[88:91], a[108:111], v[44:47]\n"
    "s_waitcnt lgkmcnt(10)\n"
    "buffer_load_dwordx4 v15, s[48:51], s72 offen offset:3072 lds\n"
    "v_mfma_f32_16x16x32_bf16 v[44:47], v[92:95], a[124:127], v[44:47]\n"
    "s_waitcnt lgkmcnt(9)\n"
    "ds_read_b128 a[180:183], v30 offset:13312\n"
    "ds_read_b128 a[136:139], v30 offset:2048\n"
    "v_mfma_f32_16x16x32_bf16 v[32:35], v[64:67], a[128:131], v[32:35]\n"
    "s_waitcnt lgkmcnt(9)\n"
    "ds_read_b64_tr_b16 v[80:81], v18 offset:8192\n"
    "ds_read_b128 a[168:171], v30 offset:10240\n"
    "v_mfma_f32_16x16x32_bf16 v[32:35], v[68:71], a[144:147], v[32:35]\n"
    "s_waitcnt lgkmcnt(9)\n"
    "s_add_u32 m0, s66, 4096\n"
    "ds_read_b64_tr_b16 v[82:83], v18 offset:10240\n"
    "ds_read_b128 a[184:187], v30 offset:14336\n"
    "v_mfma_f32_16x16x32_bf16 v[32:35], v[72:75], a[160:163], v[32:35]\n"
    "s_waitcnt lgkmcnt(10)\n"
    "buffer_load_dwordx4 v12, s[48:51], s73 offen offset:0 lds\n"
    "v_mfma_f32_16x16x32_bf16 v[32:35], v[76:79], a[176:179], v[32:35]\n"
    "s_waitcnt lgkmcnt(9)\n"
    "ds_read_b64_tr_b16 v[84:85], v18 offset:12288\n"
    "ds_read_b128 a[140:143], v30 offset:3072\n"
    "ds_read_b128 a[156:159], v30 offset:7168\n"
    "v_mfma_f32_16x16x32_bf16 v[36:39], v[64:67], a[132:135], v[36:39]\n"
    "s_waitcnt lgkmcnt(11)\n"
    "ds_read_b64_tr_b16 v[86:87], v18 offset:14336\n"
    "ds_read_b128 a[172:175], v30 offset:11264\n"
    "v_mfma_f32_16x16x32_bf16 v[36:39], v[68:71], a[148:151], v[36:39]\n"
    "s_waitcnt lgkmcnt(11)\n"
    "ds_read_b64_tr_b16 v[88:89], v18 offset:32768\n"
    "ds_read_b128 a[188:191], v30 offset:15360\n"
    "v_mfma_f32_16x16x32_bf16 v[36:39], v[72:75], a[164:167], v[36:39]\n"
    "s_waitcnt lgkmcnt(12)\n"
    "buffer_load_dwordx4 v13, s[48:51], s73 offen offset:1024 lds\n"
    "v_mfma_f32_16x16x32_bf16 v[36:39], v[76:79], a[180:183], v[36:39]\n"
    "s_waitcnt lgkmcnt(11)\n"
    "ds_read_b64_tr_b16 v[90:91], v18 offset:34816\n"
    "ds_read_b128 a[192:195], v30 offset:16384\n"
    "ds_read_b128 a[208:211], v30 offset:20480\n"
    "v_mfma_f32_16x16x32_bf16 v[40:43], v[64:67], a[136:139], v[40:43]\n"
    "ds_read_b64_tr_b16 v[92:93], v18 offset:36864\n"
    "ds_read_b128 a[224:227], v30 offset:24576\n"
    "v_mfma_f32_16x16x32_bf16 v[40:43], v[68:71], a[152:155], v[40:43]\n"
    "s_waitcnt lgkmcnt(14)\n"
    "ds_read_b64_tr_b16 v[94:95], v18 offset:38912\n"
    "ds_read_b128 a[240:243], v30 offset:28672\n"
    "v_mfma_f32_16x16x32_bf16 v[40:43], v[72:75], a[168:171], v[40:43]\n"
    "s_waitcnt lgkmcnt(14)\n"
    "buffer_load_dwordx4 v14, s[48:51], s73 offen offset:2048 lds\n"
    "v_mfma_f32_16x16x32_bf16 v[40:43], v[76:79], a[184:187], v[40:43]\n"
    "s_waitcnt lgkmcnt(12)\n"
    "ds_read_b128 a[196:199], v30 offset:17408\n"
    "ds_read_b128 a[212:215], v30 offset:21504\n"
    "v_mfma_f32_16x16x32_bf16 v[44:47], v[64:67], a[140:143], v[44:47]\n"
    "s_waitcnt lgkmcnt(13)\n"
    "ds_read_b128 a[228:231], v30 offset:25600\n"
    "ds_read_b128 a[232:235], v30 offset:26624\n"
    "v_mfma_f32_16x16x32_bf16 v[44:47], v[68:71], a[156:159], v[44:47]\n"
    "s_waitcnt lgkmcnt(13)\n"
    "ds_read_b128 a[244:247], v30 offset:29696\n"
    "v_mfma_f32_16x16x32_bf16 v[44:47], v[72:75], a[172:175], v[44:47]\n"
    "s_waitcnt lgkmcnt(12)\n"
    "buffer_load_dwordx4 v15, s[48:51], s73 offen offset:3072 lds\n"
    "v_mfma_f32_16x16x32_bf16 v[44:47], v[76:79], a[188:191], v[44:47]\n"
    "s_waitcnt vmcnt(9) lgkmcnt(6)\n"
    "ds_read_b128 a[200:203], v30 offset:18432\n"
    "ds_read_b128 a[216:219], v30 offset:22528\n"
    "s_barrier\n"
    "v_mfma_f32_16x16x32_bf16 v[32:35], v[80:83], a[192:195], v[32:35]\n"
    "ds_read_b64_tr_b16 v[64:65], v17 offset:16384\n"
    "ds_read_b128 a[248:251], v30 offset:30720\n"
    "v_mfma_f32_16x16x32_bf16 v[32:35], v[84:87], a[208:211], v[32:35]\n"
    "s_add_u32 s72, s27, 3\n"
    "s_and_b32 s72, s72, 7\n"
    "s_lshl_b32 s72, s72, 7\n"
    "s_add_u32 s72, s72, s28\n"
    "s_add_u32 s73, s72, 32768\n"
    "s_add_u32 m0, s66, 8192\n"
    "ds_read_b64_tr_b16 v[66:67], v17 offset:18432\n"
    "ds_read_b128 a[204:207], v30 offset:19456\n"
    "v_mfma_f32_16x16x32_bf16 v[32:35], v[88:91], a[224:227], v[32:35]\n"
    "s_waitcnt lgkmcnt(11)\n"
    "buffer_load_dwordx4 v12, s[48:51], s72 offen offset:0 lds\n"
    "v_mfma_f32_16x16x32_bf16 v[32:35], v[92:95], a[240:243], v[32:35]\n"
    "s_waitcnt lgkmcnt(10)\n"
    "ds_read_b64_tr_b16 v[68:69], v17 offset:20480\n"
    "ds_read_b128 a[220:223], v30 offset:23552\n"
    "ds_read_b128 a[236:239], v30 offset:27648\n"
    "v_mfma_f32_16x16x32_bf16 v[36:39], v[80:83], a[196:199], v[36:39]\n"
    "s_waitcnt lgkmcnt(12)\n"
    "ds_read_b64_tr_b16 v[70:71], v17 offset:22528\n"
    "ds_read_b128 a[252:255], v30 offset:31744\n"
    "v_mfma_f32_16x16x32_bf16 v[36:39], v[84:87], a[212:215], v[36:39]\n"
    "s_waitcnt lgkmcnt(13)\n"
    "ds_read_b64_tr_b16 v[72:73], v17 offset:40960\n"
    "v_mfma_f32_16x16x32_bf16 v[36:39], v[88:91], a[228:231], v[36:39]\n"
    "s_waitcnt lgkmcnt(12)\n"
    "buffer_load_dwordx4 v13, s[48:51], s72 offen offset:1024 lds\n"
    "v_mfma_f32_16x16x32_bf16 v[36:39], v[92:95], a[244:247], v[36:39]\n"
    "s_waitcnt lgkmcnt(11)\n"
    "ds_read_b64_tr_b16 v[74:75], v17 offset:43008\n"
    "v_mfma_f32_16x16x32_bf16 v[40:43], v[80:83], a[200:203], v[40:43]\n"
    "s_waitcnt lgkmcnt(11)\n"
    "ds_read_b64_tr_b16 v[76:77], v17 offset:45056\n"
    "v_cvt_pk_bf16_f32 v96, v32, v33\n"
    "v_mfma_f32_16x16x32_bf16 v[40:43], v[84:87], a[216:219], v[40:43]\n"
    "ds_read_b64_tr_b16 v[78:79], v17 offset:47104\n"
    "v_cvt_pk_bf16_f32 v97, v34, v35\n"
    "v_mfma_f32_16x16x32_bf16 v[40:43], v[88:91], a[232:235], v[40:43]\n"
    "s_waitcnt lgkmcnt(11)\n"
    "buffer_load_dwordx4 v14, s[48:51], s72 offen offset:2048 lds\n"
    "v_mfma_f32_16x16x32_bf16 v[40:43], v[92:95], a[248:251], v[40:43]\n"
    "s_waitcnt lgkmcnt(9)\n"
    "v_mfma_f32_16x16x32_bf16 v[44:47], v[80:83], a[204:207], v[44:47]\n"
    "s_waitcnt lgkmcnt(7)\n"
    "v_cvt_pk_bf16_f32 v98, v36, v37\n"
    "v_cvt_pk_bf16_f32 v99, v38, v39\n"
    "v_mfma_f32_16x16x32_bf16 v[44:47], v[84:87], a[220:223], v[44:47]\n"
    "s_waitcnt lgkmcnt(6)\n"
    "v_mfma_f32_16x16x32_bf16 v[44:47], v[88:91], a[236:239], v[44:47]\n"
    "s_waitcnt lgkmcnt(4)\n"
    "buffer_load_dwordx4 v15, s[48:51], s72 offen offset:3072 lds\n"
    "v_mfma_f32_16x16x32_bf16 v[44:47], v[92:95], a[252:255], v[44:47]\n"
    "v_mfma_f32_16x16x32_bf16 v[48:51], v[64:67], a[0:3], 0\n"
    "ds_read_b64_tr_b16 v[80:81], v18 offset:16384\n"
    "v_cvt_pk_bf16_f32 v100, v40, v41\n"
    "v_mfma_f32_16x16x32_bf16 v[48:51], v[68:71], a[16:19], v[48:51]\n"
    "s_waitcnt lgkmcnt(3)\n"
    "s_add_u32 m0, s66, 12288\n"
    "ds_read_b64_tr_b16 v[82:83], v18 offset:18432\n"
    "v_cvt_pk_bf16_f32 v101, v42, v43\n"
    "v_mfma_f32_16x16x32_bf16 v[48:51], v[72:75], a[32:35], v[48:51]\n"
    "s_waitcnt lgkmcnt(2)\n"
    "buffer_load_dwordx4 v12, s[48:51], s73 offen offset:0 lds\n"
    "v_mfma_f32_16x16x32_bf16 v[48:51], v[76:79], a[48:51], v[48:51]\n"
    "ds_read_b64_tr_b16 v[84:85], v18 offset:20480\n"
    "v_mfma_f32_16x16x32_bf16 v[52:55], v[64:67], a[4:7], 0\n"
    "ds_read_b64_tr_b16 v[86:87], v18 offset:22528\n"
    "v_cvt_pk_bf16_f32 v102, v44, v45\n"
    "v_mfma_f32_16x16x32_bf16 v[52:55], v[68:71], a[20:23], v[52:55]\n"
    "ds_read_b64_tr_b16 v[88:89], v18 offset:40960\n"
    "v_cvt_pk_bf16_f32 v103, v46, v47\n"
    "v_mfma_f32_16x16x32_bf16 v[52:55], v[72:75], a[36:39], v[52:55]\n"
    "buffer_load_dwordx4 v13, s[48:51], s73 offen offset:1024 lds\n"
    "v_mfma_f32_16x16x32_bf16 v[52:55], v[76:79], a[52:55], v[52:55]\n"
    "ds_read_b64_tr_b16 v[90:91], v18 offset:43008\n"
    "v_mfma_f32_16x16x32_bf16 v[56:59], v[64:67], a[8:11], 0\n"
    "ds_read_b64_tr_b16 v[92:93], v18 offset:45056\n"
    "v_mfma_f32_16x16x32_bf16 v[56:59], v[68:71], a[24:27], v[56:59]\n"
    "ds_read_b64_tr_b16 v[94:95], v18 offset:47104\n"
    "v_mfma_f32_16x16x32_bf16 v[56:59], v[72:75], a[40:43], v[56:59]\n"
    "buffer_load_dwordx4 v14, s[48:51], s73 offen offset:2048 lds\n"
    "v_mfma_f32_16x16x32_bf16 v[56:59], v[76:79], a[56:59], v[56:59]\n"
    "v_mfma_f32_16x16x32_bf16 v[60:63], v[64:67], a[12:15], 0\n"
    "v_mfma_f32_16x16x32_bf16 v[60:63], v[68:71], a[28:31], v[60:63]\n"
    "v_mfma_f32_16x16x32_bf16 v[60:63], v[72:75], a[44:47], v[60:63]\n"
    "buffer_load_dwordx4 v15, s[48:51], s73 offen offset:3072 lds\n"
    "v_mfma_f32_16x16x32_bf16 v[60:63], v[76:79], a[60:63], v[60:63]\n"
    "s_waitcnt vmcnt(8) lgkmcnt(0)\n"
    "s_barrier\n"
    "v_mfma_f32_16x16x32_bf16 v[48:51], v[80:83], a[64:67], v[48:51]\n"
    "ds_read_b64_tr_b16 v[64:65], v17 offset:0\n"
    "v_mov_b32 v219, 0x3f80\n"
    "v_mfma_f32_16x16x32_bf16 v[48:51], v[84:87], a[80:83], v[48:51]\n"
    "s_xor_b32 s72, s72, 262144\n"
    "s_add_u32 s73, s72, 32768\n"
    "s_add_u32 m0, s77, 0\n"
    "ds_read_b64_tr_b16 v[66:67], v17 offset:2048\n"
    "v_and_b32 v216, 15, v0\n"
    "v_mfma_f32_16x16x32_bf16 v[48:51], v[88:91], a[96:99], v[48:51]\n"
    "buffer_load_dwordx4 v12, s[48:51], s72 offen offset:0 lds\n"
    "v_mfma_f32_16x16x32_bf16 v[48:51], v[92:95], a[112:115], v[48:51]\n"
    "ds_read_b64_tr_b16 v[68:69], v17 offset:4096\n"
    "v_lshrrev_b32 v217, 4, v0\n"
    "v_mfma_f32_16x16x32_bf16 v[52:55], v[80:83], a[68:71], v[52:55]\n"
    "ds_read_b64_tr_b16 v[70:71], v17 offset:6144\n"
    "v_lshrrev_b32 v218, 2, v216\n"
    "v_mfma_f32_16x16x32_bf16 v[52:55], v[84:87], a[84:87], v[52:55]\n"
    "ds_read_b64_tr_b16 v[72:73], v17 offset:24576\n"
    "v_cmp_eq_u32 vcc, v218, v217\n"
    "v_mfma_f32_16x16x32_bf16 v[52:55], v[88:91], a[100:103], v[52:55]\n"
    "buffer_load_dwordx4 v13, s[48:51], s72 offen offset:1024 lds\n"
    "v_mfma_f32_16x16x32_bf16 v[52:55], v[92:95], a[116:119], v[52:55]\n"
    "ds_read_b64_tr_b16 v[74:75], v17 offset:26624\n"
    "v_and_b32 v218, 1, v0\n"
    "v_mfma_f32_16x16x32_bf16 v[56:59], v[80:83], a[72:75], v[56:59]\n"
    "ds_read_b64_tr_b16 v[76:77], v17 offset:28672\n"
    "v_lshlrev_b32 v218, 4, v218\n"
    "v_mfma_f32_16x16x32_bf16 v[56:59], v[84:87], a[88:91], v[56:59]\n"
    "ds_read_b64_tr_b16 v[78:79], v17 offset:30720\n"
    "v_lshlrev_b32 v218, v218, v219\n"
    "v_mfma_f32_16x16x32_bf16 v[56:59], v[88:91], a[104:107], v[56:59]\n"
    "buffer_load_dwordx4 v14, s[48:51], s72 offen offset:2048 lds\n"
    "v_mfma_f32_16x16x32_bf16 v[56:59], v[92:95], a[120:123], v[56:59]\n"
    "v_cndmask_b32 v218, 0, v218, vcc\n"
    "v_and_b32 v219, 2, v0\n"
    "v_mfma_f32_16x16x32_bf16 v[60:63], v[80:83], a[76:79], v[60:63]\n"
    "v_cmp_eq_u32 vcc, 0, v219\n"
    "s_nop 1\n"
    "v_cndmask_b32 v208, 0, v218, vcc\n"
    "v_mfma_f32_16x16x32_bf16 v[60:63], v[84:87], a[92:95], v[60:63]\n"
    "v_sub_u32 v209, v218, v208\n"
    "v_mov_b32 v210, 0\n"
    "v_mfma_f32_16x16x32_bf16 v[60:63], v[88:91], a[108:111], v[60:63]\n"
    "buffer_load_dwordx4 v15, s[48:51], s72 offen offset:3072 lds\n"
    "v_mfma_f32_16x16x32_bf16 v[60:63], v[92:95], a[124:127], v[60:63]\n"
    "s_waitcnt lgkmcnt(6)\n"
    "v_mov_b32 v211, 0\n"
    "v_mov_b32 v212, 0\n"
    "v_mfma_f32_16x16x32_bf16 v[48:51], v[64:67], a[128:131], v[48:51]\n"
    "s_waitcnt lgkmcnt(4)\n"
    "ds_read_b64_tr_b16 v[80:81], v18 offset:0\n"
    "v_mov_b32 v213, 0\n"
    "v_mfma_f32_16x16x32_bf16 v[48:51], v[68:71], a[144:147], v[48:51]\n"
    "s_waitcnt lgkmcnt(3)\n"
    "s_add_u32 m0, s77, 4096\n"
    "ds_read_b64_tr_b16 v[82:83], v18 offset:2048\n"
    "v_mov_b32 v214, v208\n"
    "v_mfma_f32_16x16x32_bf16 v[48:51], v[72:75], a[160:163], v[48:51]\n"
    "s_waitcnt lgkmcnt(2)\n"
    "buffer_load_dwordx4 v12, s[48:51], s73 offen offset:0 lds\n"
    "v_mfma_f32_16x16x32_bf16 v[48:51], v[76:79], a[176:179], v[48:51]\n"
    "ds_read_b64_tr_b16 v[84:85], v18 offset:4096\n"
    "v_mov_b32 v215, v209\n"
    "v_mfma_f32_16x16x32_bf16 v[52:55], v[64:67], a[132:135], v[52:55]\n"
    "ds_read_b64_tr_b16 v[86:87], v18 offset:6144\n"
    "v_and_b32 v218, 1, v217\n"
    "v_mfma_f32_16x16x32_bf16 v[52:55], v[68:71], a[148:151], v[52:55]\n"
    "ds_read_b64_tr_b16 v[88:89], v18 offset:24576\n"
    "v_lshl_add_u32 v216, v218, 4, v216\n"
    "v_mfma_f32_16x16x32_bf16 v[52:55], v[72:75], a[164:167], v[52:55]\n"
    "buffer_load_dwordx4 v13, s[48:51], s73 offen offset:1024 lds\n"
    "v_mfma_f32_16x16x32_bf16 v[52:55], v[76:79], a[180:183], v[52:55]\n"
    "ds_read_b64_tr_b16 v[90:91], v18 offset:26624\n"
    "v_lshrrev_b32 v217, 1, v217\n"
    "v_mfma_f32_16x16x32_bf16 v[56:59], v[64:67], a[136:139], v[56:59]\n"
    "ds_read_b64_tr_b16 v[92:93], v18 offset:28672\n"
    "v_lshl_add_u32 v217, s60, 1, v217\n"
    "v_mfma_f32_16x16x32_bf16 v[56:59], v[68:71], a[152:155], v[56:59]\n"
    "ds_read_b64_tr_b16 v[94:95], v18 offset:30720\n"
    "v_and_b32 v218, 7, v216\n"
    "v_mfma_f32_16x16x32_bf16 v[56:59], v[72:75], a[168:171], v[56:59]\n"
    "buffer_load_dwordx4 v14, s[48:51], s73 offen offset:2048 lds\n"
    "v_mfma_f32_16x16x32_bf16 v[56:59], v[76:79], a[184:187], v[56:59]\n"
    "v_xor_b32 v217, v217, v218\n"
    "v_lshlrev_b32 v217, 4, v217\n"
    "v_mfma_f32_16x16x32_bf16 v[60:63], v[64:67], a[140:143], v[60:63]\n"
    "v_lshl_add_u32 v217, v216, 7, v217\n"
    "v_add_u32 v27, 0x8000, v217\n"
    "v_mfma_f32_16x16x32_bf16 v[60:63], v[68:71], a[156:159], v[60:63]\n"
    "v_lshrrev_b32 v216, 3, v0\n"
    "v_and_b32 v217, 7, v0\n"
    "v_mfma_f32_16x16x32_bf16 v[60:63], v[72:75], a[172:175], v[60:63]\n"
    "buffer_load_dwordx4 v15, s[48:51], s73 offen offset:3072 lds\n"
    "v_mfma_f32_16x16x32_bf16 v[60:63], v[76:79], a[188:191], v[60:63]\n"
    "s_waitcnt vmcnt(8) lgkmcnt(0)\n"
    "v_lshlrev_b32 v218, 4, v217\n"
    "v_lshl_add_u32 v221, v216, 11, v218\n"
    "s_barrier\n"
    "v_mfma_f32_16x16x32_bf16 v[48:51], v[80:83], a[192:195], v[48:51]\n"
    "ds_read_b64_tr_b16 v[64:65], v17 offset:8192\n"
    "v_xor_b32 v217, v217, v216\n"
    "v_mfma_f32_16x16x32_bf16 v[48:51], v[84:87], a[208:211], v[48:51]\n"
    "s_add_u32 s72, s27, 4\n"
    "s_and_b32 s72, s72, 7\n"
    "s_lshl_b32 s72, s72, 7\n"
    "s_add_u32 s72, s72, s28\n"
    "s_add_u32 s73, s72, 32768\n"
    "s_add_u32 m0, s77, 32768\n"
    "ds_read_b64_tr_b16 v[66:67], v17 offset:10240\n"
    "v_lshlrev_b32 v217, 4, v217\n"
    "v_mfma_f32_16x16x32_bf16 v[48:51], v[88:91], a[224:227], v[48:51]\n"
    "buffer_load_dwordx4 v12, s[48:51], s72 offen offset:0 lds\n"
    "v_mfma_f32_16x16x32_bf16 v[48:51], v[92:95], a[240:243], v[48:51]\n"
    "ds_read_b64_tr_b16 v[68:69], v17 offset:12288\n"
    "v_lshl_add_u32 v217, v216, 7, v217\n"
    "v_mfma_f32_16x16x32_bf16 v[52:55], v[80:83], a[196:199], v[52:55]\n"
    "ds_read_b64_tr_b16 v[70:71], v17 offset:14336\n"
    "v_add_u32 v217, s90, v217\n"
    "v_mfma_f32_16x16x32_bf16 v[52:55], v[84:87], a[212:215], v[52:55]\n"
    "ds_read_b64_tr_b16 v[72:73], v17 offset:32768\n"
    "v_add_u32 v220, 0x8000, v217\n"
    "v_mfma_f32_16x16x32_bf16 v[52:55], v[88:91], a[228:231], v[52:55]\n"
    "buffer_load_dwordx4 v13, s[48:51], s72 offen offset:1024 lds\n"
    "v_mfma_f32_16x16x32_bf16 v[52:55], v[92:95], a[244:247], v[52:55]\n"
    "ds_read_b64_tr_b16 v[74:75], v17 offset:34816\n"
    "v_mfma_f32_16x16x32_bf16 v[56:59], v[80:83], a[200:203], v[56:59]\n"
    "ds_read_b64_tr_b16 v[76:77], v17 offset:36864\n"
    "v_cvt_pk_bf16_f32 v104, v48, v49\n"
    "v_mfma_f32_16x16x32_bf16 v[56:59], v[84:87], a[216:219], v[56:59]\n"
    "ds_read_b64_tr_b16 v[78:79], v17 offset:38912\n"
    "v_cvt_pk_bf16_f32 v105, v50, v51\n"
    "v_mfma_f32_16x16x32_bf16 v[56:59], v[88:91], a[232:235], v[56:59]\n"
    "buffer_load_dwordx4 v14, s[48:51], s72 offen offset:2048 lds\n"
    "v_mfma_f32_16x16x32_bf16 v[56:59], v[92:95], a[248:251], v[56:59]\n"
    "v_mfma_f32_16x16x32_bf16 v[60:63], v[80:83], a[204:207], v[60:63]\n"
    "v_cvt_pk_bf16_f32 v106, v52, v53\n"
    "v_cvt_pk_bf16_f32 v107, v54, v55\n"
    "v_mfma_f32_16x16x32_bf16 v[60:63], v[84:87], a[220:223], v[60:63]\n"
    "v_mfma_f32_16x16x32_bf16 v[60:63], v[88:91], a[236:239], v[60:63]\n"
    "buffer_load_dwordx4 v15, s[48:51], s72 offen offset:3072 lds\n"
    "v_mfma_f32_16x16x32_bf16 v[60:63], v[92:95], a[252:255], v[60:63]\n"
    "s_waitcnt lgkmcnt(6)\n"
    "v_mfma_f32_16x16x32_bf16 v[32:35], v[64:67], a[0:3], 0\n"
    "s_waitcnt lgkmcnt(4)\n"
    "ds_read_b64_tr_b16 v[80:81], v18 offset:8192\n"
    "v_cvt_pk_bf16_f32 v108, v56, v57\n"
    "v_mfma_f32_16x16x32_bf16 v[32:35], v[68:71], a[16:19], v[32:35]\n"
    "s_waitcnt lgkmcnt(3)\n"
    "s_add_u32 m0, s77, 36864\n"
    "ds_read_b64_tr_b16 v[82:83], v18 offset:10240\n"
    "v_cvt_pk_bf16_f32 v109, v58, v59\n"
    "v_mfma_f32_16x16x32_bf16 v[32:35], v[72:75], a[32:35], v[32:35]\n"
    "s_waitcnt lgkmcnt(2)\n"
    "buffer_load_dwordx4 v12, s[48:51], s73 offen offset:0 lds\n"
    "v_mfma_f32_16x16x32_bf16 v[32:35], v[76:79], a[48:51], v[32:35]\n"
    "ds_read_b64_tr_b16 v[84:85], v18 offset:12288\n"
    "v_mfma_f32_16x16x32_bf16 v[36:39], v[64:67], a[4:7], 0\n"
    "ds_read_b64_tr_b16 v[86:87], v18 offset:14336\n"
    "v_cvt_pk_bf16_f32 v110, v60, v61\n"
    "v_mfma_f32_16x16x32_bf16 v[36:39], v[68:71], a[20:23], v[36:39]\n"
    "ds_read_b64_tr_b16 v[88:89], v18 offset:32768\n"
    "v_cvt_pk_bf16_f32 v111, v62, v63\n"
    "v_mfma_f32_16x16x32_bf16 v[36:39], v[72:75], a[36:39], v[36:39]\n"
    "buffer_load_dwordx4 v13, s[48:51], s73 offen offset:1024 lds\n"
    "v_mfma_f32_16x16x32_bf16 v[36:39], v[76:79], a[52:55], v[36:39]\n"
    "ds_read_b64_tr_b16 v[90:91], v18 offset:34816\n"
    "v_mfma_f32_16x16x32_bf16 v[40:43], v[64:67], a[8:11], 0\n"
    "ds_read_b64_tr_b16 v[92:93], v18 offset:36864\n"
    "v_mfma_f32_16x16x32_bf16 v[40:43], v[68:71], a[24:27], v[40:43]\n"
    "ds_read_b64_tr_b16 v[94:95], v18 offset:38912\n"
    "v_mfma_f32_16x16x32_bf16 v[40:43], v[72:75], a[40:43], v[40:43]\n"
    "buffer_load_dwordx4 v14, s[48:51], s73 offen offset:2048 lds\n"
    "v_mfma_f32_16x16x32_bf16 v[40:43], v[76:79], a[56:59], v[40:43]\n"
    "v_mfma_f32_16x16x32_bf16 v[44:47], v[64:67], a[12:15], 0\n"
    "v_mfma_f32_16x16x32_bf16 v[44:47], v[68:71], a[28:31], v[44:47]\n"
    "v_mfma_f32_16x16x32_bf16 v[44:47], v[72:75], a[44:47], v[44:47]\n"
    "buffer_load_dwordx4 v15, s[48:51], s73 offen offset:3072 lds\n"
    "v_mfma_f32_16x16x32_bf16 v[44:47], v[76:79], a[60:63], v[44:47]\n"
    "s_waitcnt vmcnt(8) lgkmcnt(0)\n"
    "buffer_store_dwordx4 v[96:99], v1, s[52:55], s12 offen sc1\n"
    "s_barrier\n"
    "v_mfma_f32_16x16x32_bf16 v[32:35], v[80:83], a[64:67], v[32:35]\n"
    "ds_read_b64_tr_b16 v[64:65], v16 offset:0\n"
    "v_mfma_f32_16x16x32_bf16 v[32:35], v[84:87], a[80:83], v[32:35]\n"
    "s_xor_b32 s72, s72, 262144\n"
    "s_add_u32 s73, s72, 32768\n"
    "s_add_u32 m0, s66, 16384\n"
    "ds_read_b64_tr_b16 v[66:67], v16 offset:2048\n"
    "v_mfma_f32_16x16x32_bf16 v[32:35], v[88:91], a[96:99], v[32:35]\n"
    "buffer_load_dwordx4 v12, s[48:51], s72 offen offset:0 lds\n"
    "v_mfma_f32_16x16x32_bf16 v[32:35], v[92:95], a[112:115], v[32:35]\n"
    "ds_read_b64_tr_b16 v[68:69], v16 offset:4096\n"
    "v_mfma_f32_16x16x32_bf16 v[36:39], v[80:83], a[68:71], v[36:39]\n"
    "ds_read_b64_tr_b16 v[70:71], v16 offset:6144\n"
    "v_mfma_f32_16x16x32_bf16 v[36:39], v[84:87], a[84:87], v[36:39]\n"
    "ds_read_b64_tr_b16 v[72:73], v16 offset:8192\n"
    "v_mfma_f32_16x16x32_bf16 v[36:39], v[88:91], a[100:103], v[36:39]\n"
    "buffer_load_dwordx4 v13, s[48:51], s72 offen offset:1024 lds\n"
    "v_mfma_f32_16x16x32_bf16 v[36:39], v[92:95], a[116:119], v[36:39]\n"
    "ds_read_b64_tr_b16 v[74:75], v16 offset:10240\n"
    "v_mfma_f32_16x16x32_bf16 v[40:43], v[80:83], a[72:75], v[40:43]\n"
    "ds_read_b64_tr_b16 v[76:77], v16 offset:12288\n"
    "v_mfma_f32_16x16x32_bf16 v[40:43], v[84:87], a[88:91], v[40:43]\n"
    "ds_read_b64_tr_b16 v[78:79], v16 offset:14336\n"
    "v_mfma_f32_16x16x32_bf16 v[40:43], v[88:91], a[104:107], v[40:43]\n"
    "buffer_load_dwordx4 v14, s[48:51], s72 offen offset:2048 lds\n"
    "v_mfma_f32_16x16x32_bf16 v[40:43], v[92:95], a[120:123], v[40:43]\n"
    "buffer_store_dwordx4 v[100:103], v1, s[52:55], s12 offen offset:1024 sc1\n"
    "v_mfma_f32_16x16x32_bf16 v[44:47], v[80:83], a[76:79], v[44:47]\n"
    "v_mfma_f32_16x16x32_bf16 v[44:47], v[84:87], a[92:95], v[44:47]\n"
    "v_mfma_f32_16x16x32_bf16 v[44:47], v[88:91], a[108:111], v[44:47]\n"
    "buffer_load_dwordx4 v15, s[48:51], s72 offen offset:3072 lds\n"
    "v_mfma_f32_16x16x32_bf16 v[44:47], v[92:95], a[124:127], v[44:47]\n"
    "s_waitcnt lgkmcnt(6)\n"
    "buffer_store_dwordx4 v[104:107], v1, s[52:55], s13 offen sc1\n"
    "v_mfma_f32_16x16x32_bf16 v[32:35], v[64:67], a[128:131], v[32:35]\n"
    "s_waitcnt lgkmcnt(4)\n"
    "ds_read_b64_tr_b16 v[80:81], v16 offset:16384\n"
    "v_mfma_f32_16x16x32_bf16 v[32:35], v[68:71], a[144:147], v[32:35]\n"
    "s_waitcnt lgkmcnt(3)\n"
    "s_add_u32 m0, s66, 20480\n"
    "ds_read_b64_tr_b16 v[82:83], v16 offset:18432\n"
    "v_mfma_f32_16x16x32_bf16 v[32:35], v[72:75], a[160:163], v[32:35]\n"
    "s_waitcnt lgkmcnt(2)\n"
    "buffer_load_dwordx4 v12, s[48:51], s73 offen offset:0 lds\n"
    "v_mfma_f32_16x16x32_bf16 v[32:35], v[76:79], a[176:179], v[32:35]\n"
    "ds_read_b64_tr_b16 v[84:85], v16 offset:20480\n"
    "v_mfma_f32_16x16x32_bf16 v[36:39], v[64:67], a[132:135], v[36:39]\n"
    "ds_read_b64_tr_b16 v[86:87], v16 offset:22528\n"
    "v_mfma_f32_16x16x32_bf16 v[36:39], v[68:71], a[148:151], v[36:39]\n"
    "ds_read_b64_tr_b16 v[88:89], v16 offset:24576\n"
    "v_mfma_f32_16x16x32_bf16 v[36:39], v[72:75], a[164:167], v[36:39]\n"
    "buffer_load_dwordx4 v13, s[48:51], s73 offen offset:1024 lds\n"
    "v_mfma_f32_16x16x32_bf16 v[36:39], v[76:79], a[180:183], v[36:39]\n"
    "ds_read_b64_tr_b16 v[90:91], v16 offset:26624\n"
    "v_mfma_f32_16x16x32_bf16 v[40:43], v[64:67], a[136:139], v[40:43]\n"
    "ds_read_b64_tr_b16 v[92:93], v16 offset:28672\n"
    "v_mfma_f32_16x16x32_bf16 v[40:43], v[68:71], a[152:155], v[40:43]\n"
    "ds_read_b64_tr_b16 v[94:95], v16 offset:30720\n"
    "v_mfma_f32_16x16x32_bf16 v[40:43], v[72:75], a[168:171], v[40:43]\n"
    "buffer_load_dwordx4 v14, s[48:51], s73 offen offset:2048 lds\n"
    "v_mfma_f32_16x16x32_bf16 v[40:43], v[76:79], a[184:187], v[40:43]\n"
    "buffer_store_dwordx4 v[108:111], v1, s[52:55], s13 offen offset:1024 sc1\n"
    "v_mfma_f32_16x16x32_bf16 v[44:47], v[64:67], a[140:143], v[44:47]\n"
    "v_mfma_f32_16x16x32_bf16 v[44:47], v[68:71], a[156:159], v[44:47]\n"
    "v_mfma_f32_16x16x32_bf16 v[44:47], v[72:75], a[172:175], v[44:47]\n"
    "buffer_load_dwordx4 v15, s[48:51], s73 offen offset:3072 lds\n"
    "v_mfma_f32_16x16x32_bf16 v[44:47], v[76:79], a[188:191], v[44:47]\n"
    "s_waitcnt vmcnt(12) lgkmcnt(0)\n"
    "buffer_load_dword v24, v22, s[52:55], 0 offen sc1\n"
    "s_barrier\n"
    "v_mfma_f32_16x16x32_bf16 v[32:35], v[80:83], a[192:195], v[32:35]\n"
    "ds_read_b64_tr_b16 v[64:65], v16 offset:32768\n"
    "v_mfma_f32_16x16x32_bf16 v[32:35], v[84:87], a[208:211], v[32:35]\n"
    "s_add_u32 s72, s27, 5\n"
    "s_and_b32 s72, s72, 7\n"
    "s_lshl_b32 s72, s72, 7\n"
    "s_add_u32 s72, s72, s28\n"
    "s_add_u32 s73, s72, 32768\n"
    "s_add_u32 m0, s66, 0\n"
    "ds_read_b64_tr_b16 v[66:67], v16 offset:34816\n"
    "v_mfma_f32_16x16x32_bf16 v[32:35], v[88:91], a[224:227], v[32:35]\n"
    "buffer_load_dwordx4 v12, s[48:51], s72 offen offset:0 lds\n"
    "v_mfma_f32_16x16x32_bf16 v[32:35], v[92:95], a[240:243], v[32:35]\n"
    "ds_read_b64_tr_b16 v[68:69], v16 offset:36864\n"
    "v_mfma_f32_16x16x32_bf16 v[36:39], v[80:83], a[196:199], v[36:39]\n"
    "ds_read_b64_tr_b16 v[70:71], v16 offset:38912\n"
    "v_mfma_f32_16x16x32_bf16 v[36:39], v[84:87], a[212:215], v[36:39]\n"
    "ds_read_b64_tr_b16 v[72:73], v16 offset:40960\n"
    "v_mfma_f32_16x16x32_bf16 v[36:39], v[88:91], a[228:231], v[36:39]\n"
    "buffer_load_dwordx4 v13, s[48:51], s72 offen offset:1024 lds\n"
    "v_mfma_f32_16x16x32_bf16 v[36:39], v[92:95], a[244:247], v[36:39]\n"
    "ds_read_b64_tr_b16 v[74:75], v16 offset:43008\n"
    "v_mfma_f32_16x16x32_bf16 v[40:43], v[80:83], a[200:203], v[40:43]\n"
    "ds_read_b64_tr_b16 v[76:77], v16 offset:45056\n"
    "v_cvt_pk_bf16_f32 v112, v32, v33\n"
    "v_mfma_f32_16x16x32_bf16 v[40:43], v[84:87], a[216:219], v[40:43]\n"
    "ds_read_b64_tr_b16 v[78:79], v16 offset:47104\n"
    "v_cvt_pk_bf16_f32 v113, v34, v35\n"
    "v_mfma_f32_16x16x32_bf16 v[40:43], v[88:91], a[232:235], v[40:43]\n"
    "buffer_load_dwordx4 v14, s[48:51], s72 offen offset:2048 lds\n"
    "v_mfma_f32_16x16x32_bf16 v[40:43], v[92:95], a[248:251], v[40:43]\n"
    "v_mfma_f32_16x16x32_bf16 v[44:47], v[80:83], a[204:207], v[44:47]\n"
    "v_cvt_pk_bf16_f32 v114, v36, v37\n"
    "v_cvt_pk_bf16_f32 v115, v38, v39\n"
    "v_mfma_f32_16x16x32_bf16 v[44:47], v[84:87], a[220:223], v[44:47]\n"
    "v_mfma_f32_16x16x32_bf16 v[44:47], v[88:91], a[236:239], v[44:47]\n"
    "buffer_load_dwordx4 v15, s[48:51], s72 offen offset:3072 lds\n"
    "v_mfma_f32_16x16x32_bf16 v[44:47], v[92:95], a[252:255], v[44:47]\n"
    "s_waitcnt lgkmcnt(6)\n"
    "v_mfma_f32_16x16x32_bf16 v[48:51], v[64:67], a[0:3], 0\n"
    "s_waitcnt lgkmcnt(4)\n"
    "ds_read_b64_tr_b16 v[80:81], v16 offset:49152\n"
    "v_cvt_pk_bf16_f32 v116, v40, v41\n"
    "v_mfma_f32_16x16x32_bf16 v[48:51], v[68:71], a[16:19], v[48:51]\n"
    "s_waitcnt lgkmcnt(3)\n"
    "s_add_u32 m0, s66, 4096\n"
    "ds_read_b64_tr_b16 v[82:83], v16 offset:51200\n"
    "v_cvt_pk_bf16_f32 v117, v42, v43\n"
    "v_mfma_f32_16x16x32_bf16 v[48:51], v[72:75], a[32:35], v[48:51]\n"
    "s_waitcnt lgkmcnt(2)\n"
    "buffer_load_dwordx4 v12, s[48:51], s73 offen offset:0 lds\n"
    "v_mfma_f32_16x16x32_bf16 v[48:51], v[76:79], a[48:51], v[48:51]\n"
    "ds_read_b64_tr_b16 v[84:85], v16 offset:53248\n"
    "v_mfma_f32_16x16x32_bf16 v[52:55], v[64:67], a[4:7], 0\n"
    "ds_read_b64_tr_b16 v[86:87], v16 offset:55296\n"
    "v_cvt_pk_bf16_f32 v118, v44, v45\n"
    "v_mfma_f32_16x16x32_bf16 v[52:55], v[68:71], a[20:23], v[52:55]\n"
    "ds_read_b64_tr_b16 v[88:89], v16 offset:57344\n"
    "v_cvt_pk_bf16_f32 v119, v46, v47\n"
    "v_mfma_f32_16x16x32_bf16 v[52:55], v[72:75], a[36:39], v[52:55]\n"
    "buffer_load_dwordx4 v13, s[48:51], s73 offen offset:1024 lds\n"
    "v_mfma_f32_16x16x32_bf16 v[52:55], v[76:79], a[52:55], v[52:55]\n"
    "ds_read_b64_tr_b16 v[90:91], v16 offset:59392\n"
    "v_mfma_f32_16x16x32_bf16 v[56:59], v[64:67], a[8:11], 0\n"
    "ds_read_b64_tr_b16 v[92:93], v16 offset:61440\n"
    "v_mfma_f32_16x16x32_bf16 v[56:59], v[68:71], a[24:27], v[56:59]\n"
    "ds_read_b64_tr_b16 v[94:95], v16 offset:63488\n"
    "v_mfma_f32_16x16x32_bf16 v[56:59], v[72:75], a[40:43], v[56:59]\n"
    "buffer_load_dwordx4 v14, s[48:51], s73 offen offset:2048 lds\n"
    "v_mfma_f32_16x16x32_bf16 v[56:59], v[76:79], a[56:59], v[56:59]\n"
    "v_mfma_f32_16x16x32_bf16 v[60:63], v[64:67], a[12:15], 0\n"
    "v_mfma_f32_16x16x32_bf16 v[60:63], v[68:71], a[28:31], v[60:63]\n"
    "v_mfma_f32_16x16x32_bf16 v[60:63], v[72:75], a[44:47], v[60:63]\n"
    "buffer_load_dwordx4 v15, s[48:51], s73 offen offset:3072 lds\n"
    "v_mfma_f32_16x16x32_bf16 v[60:63], v[76:79], a[60:63], v[60:63]\n"
    "s_waitcnt vmcnt(9) lgkmcnt(0)\n"
    "s_barrier\n"
    "v_mfma_f32_16x16x32_bf16 v[48:51], v[80:83], a[64:67], v[48:51]\n"
    "ds_read_b64_tr_b16 v[64:65], v17 offset:16384\n"
    "v_mfma_f32_16x16x32_bf16 v[48:51], v[84:87], a[80:83], v[48:51]\n"
    "s_xor_b32 s72, s72, 262144\n"
    "s_add_u32 s73, s72, 32768\n"
    "s_add_u32 m0, s66, 8192\n"
    "ds_read_b64_tr_b16 v[66:67], v17 offset:18432\n"
    "v_mfma_f32_16x16x32_bf16 v[48:51], v[88:91], a[96:99], v[48:51]\n"
    "buffer_load_dwordx4 v12, s[48:51], s72 offen offset:0 lds\n"
    "v_mfma_f32_16x16x32_bf16 v[48:51], v[92:95], a[112:115], v[48:51]\n"
    "ds_read_b64_tr_b16 v[68:69], v17 offset:20480\n"
    "v_mfma_f32_16x16x32_bf16 v[52:55], v[80:83], a[68:71], v[52:55]\n"
    "ds_read_b64_tr_b16 v[70:71], v17 offset:22528\n"
    "v_mfma_f32_16x16x32_bf16 v[52:55], v[84:87], a[84:87], v[52:55]\n"
    "ds_read_b64_tr_b16 v[72:73], v17 offset:40960\n"
    "v_mfma_f32_16x16x32_bf16 v[52:55], v[88:91], a[100:103], v[52:55]\n"
    "buffer_load_dwordx4 v13, s[48:51], s72 offen offset:1024 lds\n"
    "v_mfma_f32_16x16x32_bf16 v[52:55], v[92:95], a[116:119], v[52:55]\n"
    "ds_read_b64_tr_b16 v[74:75], v17 offset:43008\n"
    "v_mfma_f32_16x16x32_bf16 v[56:59], v[80:83], a[72:75], v[56:59]\n"
    "ds_read_b64_tr_b16 v[76:77], v17 offset:45056\n"
    "v_mfma_f32_16x16x32_bf16 v[56:59], v[84:87], a[88:91], v[56:59]\n"
    "ds_read_b64_tr_b16 v[78:79], v17 offset:47104\n"
    "v_mfma_f32_16x16x32_bf16 v[56:59], v[88:91], a[104:107], v[56:59]\n"
    "buffer_load_dwordx4 v14, s[48:51], s72 offen offset:2048 lds\n"
    "v_mfma_f32_16x16x32_bf16 v[56:59], v[92:95], a[120:123], v[56:59]\n"
    "v_mfma_f32_16x16x32_bf16 v[60:63], v[80:83], a[76:79], v[60:63]\n"
    "v_mfma_f32_16x16x32_bf16 v[60:63], v[84:87], a[92:95], v[60:63]\n"
    "v_mfma_f32_16x16x32_bf16 v[60:63], v[88:91], a[108:111], v[60:63]\n"
    "buffer_load_dwordx4 v15, s[48:51], s72 offen offset:3072 lds\n"
    "v_mfma_f32_16x16x32_bf16 v[60:63], v[92:95], a[124:127], v[60:63]\n"
    "s_waitcnt lgkmcnt(6)\n"
    "v_mfma_f32_16x16x32_bf16 v[48:51], v[64:67], a[128:131], v[48:51]\n"
    "s_waitcnt lgkmcnt(4)\n"
    "ds_read_b64_tr_b16 v[80:81], v18 offset:16384\n"
    "v_mfma_f32_16x16x32_bf16 v[48:51], v[68:71], a[144:147], v[48:51]\n"
    "s_waitcnt lgkmcnt(3)\n"
    "s_add_u32 m0, s66, 12288\n"
    "ds_read_b64_tr_b16 v[82:83], v18 offset:18432\n"
    "v_mfma_f32_16x16x32_bf16 v[48:51], v[72:75], a[160:163], v[48:51]\n"
    "s_waitcnt lgkmcnt(2)\n"
    "buffer_load_dwordx4 v12, s[48:51], s73 offen offset:0 lds\n"
    "v_mfma_f32_16x16x32_bf16 v[48:51], v[76:79], a[176:179], v[48:51]\n"
    "ds_read_b64_tr_b16 v[84:85], v18 offset:20480\n"
    "v_mfma_f32_16x16x32_bf16 v[52:55], v[64:67], a[132:135], v[52:55]\n"
    "ds_read_b64_tr_b16 v[86:87], v18 offset:22528\n"
    "v_mfma_f32_16x16x32_bf16 v[52:55], v[68:71], a[148:151], v[52:55]\n"
    "ds_read_b64_tr_b16 v[88:89], v18 offset:40960\n"
    "v_mfma_f32_16x16x32_bf16 v[52:55], v[72:75], a[164:167], v[52:55]\n"
    "buffer_load_dwordx4 v13, s[48:51], s73 offen offset:1024 lds\n"
    "v_mfma_f32_16x16x32_bf16 v[52:55], v[76:79], a[180:183], v[52:55]\n"
    "ds_read_b64_tr_b16 v[90:91], v18 offset:43008\n"
    "v_mfma_f32_16x16x32_bf16 v[56:59], v[64:67], a[136:139], v[56:59]\n"
    "ds_read_b64_tr_b16 v[92:93], v18 offset:45056\n"
    "v_mfma_f32_16x16x32_bf16 v[56:59], v[68:71], a[152:155], v[56:59]\n"
    "ds_read_b64_tr_b16 v[94:95], v18 offset:47104\n"
    "v_mfma_f32_16x16x32_bf16 v[56:59], v[72:75], a[168:171], v[56:59]\n"
    "buffer_load_dwordx4 v14, s[48:51], s73 offen offset:2048 lds\n"
    "v_mfma_f32_16x16x32_bf16 v[56:59], v[76:79], a[184:187], v[56:59]\n"
    "v_mfma_f32_16x16x32_bf16 v[60:63], v[64:67], a[140:143], v[60:63]\n"
    "v_mfma_f32_16x16x32_bf16 v[60:63], v[68:71], a[156:159], v[60:63]\n"
    "v_mfma_f32_16x16x32_bf16 v[60:63], v[72:75], a[172:175], v[60:63]\n"
    "buffer_load_dwordx4 v15, s[48:51], s73 offen offset:3072 lds\n"
    "v_mfma_f32_16x16x32_bf16 v[60:63], v[76:79], a[188:191], v[60:63]\n"
    "s_waitcnt vmcnt(8) lgkmcnt(0)\n"
    "s_barrier\n"
    "v_mfma_f32_16x16x32_bf16 v[48:51], v[80:83], a[192:195], v[48:51]\n"
    "ds_read_b64_tr_b16 v[64:65], v17 offset:0\n"
    "v_mfma_f32_16x16x32_bf16 v[48:51], v[84:87], a[208:211], v[48:51]\n"
    "s_add_u32 s72, s27, 6\n"
    "s_and_b32 s72, s72, 7\n"
    "s_lshl_b32 s72, s72, 7\n"
    "s_add_u32 s72, s72, s28\n"
    "s_add_u32 s73, s72, 32768\n"
    "s_add_u32 m0, s77, 0\n"
    "ds_read_b64_tr_b16 v[66:67], v17 offset:2048\n"
    "v_mfma_f32_16x16x32_bf16 v[48:51], v[88:91], a[224:227], v[48:51]\n"
    "buffer_load_dwordx4 v12, s[48:51], s72 offen offset:0 lds\n"
    "v_mfma_f32_16x16x32_bf16 v[48:51], v[92:95], a[240:243], v[48:51]\n"
    "ds_read_b64_tr_b16 v[68:69], v17 offset:4096\n"
    "v_mfma_f32_16x16x32_bf16 v[52:55], v[80:83], a[196:199], v[52:55]\n"
    "ds_read_b64_tr_b16 v[70:71], v17 offset:6144\n"
    "v_mfma_f32_16x16x32_bf16 v[52:55], v[84:87], a[212:215], v[52:55]\n"
    "ds_read_b64_tr_b16 v[72:73], v17 offset:24576\n"
    "v_mfma_f32_16x16x32_bf16 v[52:55], v[88:91], a[228:231], v[52:55]\n"
    "buffer_load_dwordx4 v13, s[48:51], s72 offen offset:1024 lds\n"
    "v_mfma_f32_16x16x32_bf16 v[52:55], v[92:95], a[244:247], v[52:55]\n"
    "ds_read_b64_tr_b16 v[74:75], v17 offset:26624\n"
    "v_mfma_f32_16x16x32_bf16 v[56:59], v[80:83], a[200:203], v[56:59]\n"
    "ds_read_b64_tr_b16 v[76:77], v17 offset:28672\n"
    "v_cvt_pk_bf16_f32 v120, v48, v49\n"
    "v_mfma_f32_16x16x32_bf16 v[56:59], v[84:87], a[216:219], v[56:59]\n"
    "ds_read_b64_tr_b16 v[78:79], v17 offset:30720\n"
    "v_cvt_pk_bf16_f32 v121, v50, v51\n"
    "v_mfma_f32_16x16x32_bf16 v[56:59], v[88:91], a[232:235], v[56:59]\n"
    "buffer_load_dwordx4 v14, s[48:51], s72 offen offset:2048 lds\n"
    "v_mfma_f32_16x16x32_bf16 v[56:59], v[92:95], a[248:251], v[56:59]\n"
    "v_mfma_f32_16x16x32_bf16 v[60:63], v[80:83], a[204:207], v[60:63]\n"
    "v_cvt_pk_bf16_f32 v122, v52, v53\n"
    "v_cvt_pk_bf16_f32 v123, v54, v55\n"
    "v_mfma_f32_16x16x32_bf16 v[60:63], v[84:87], a[220:223], v[60:63]\n"
    "v_mfma_f32_16x16x32_bf16 v[60:63], v[88:91], a[236:239], v[60:63]\n"
    "buffer_load_dwordx4 v15, s[48:51], s72 offen offset:3072 lds\n"
    "v_mfma_f32_16x16x32_bf16 v[60:63], v[92:95], a[252:255], v[60:63]\n"
    "s_waitcnt lgkmcnt(6)\n"
    "v_mfma_f32_16x16x32_bf16 v[32:35], v[64:67], a[0:3], 0\n"
    "s_waitcnt lgkmcnt(4)\n"
    "ds_read_b64_tr_b16 v[80:81], v18 offset:0\n"
    "v_cvt_pk_bf16_f32 v124, v56, v57\n"
    "v_mfma_f32_16x16x32_bf16 v[32:35], v[68:71], a[16:19], v[32:35]\n"
    "s_waitcnt lgkmcnt(3)\n"
    "s_add_u32 m0, s77, 4096\n"
    "ds_read_b64_tr_b16 v[82:83], v18 offset:2048\n"
    "v_cvt_pk_bf16_f32 v125, v58, v59\n"
    "v_mfma_f32_16x16x32_bf16 v[32:35], v[72:75], a[32:35], v[32:35]\n"
    "s_waitcnt lgkmcnt(2)\n"
    "buffer_load_dwordx4 v12, s[48:51], s73 offen offset:0 lds\n"
    "v_mfma_f32_16x16x32_bf16 v[32:35], v[76:79], a[48:51], v[32:35]\n"
    "ds_read_b64_tr_b16 v[84:85], v18 offset:4096\n"
    "v_mfma_f32_16x16x32_bf16 v[36:39], v[64:67], a[4:7], 0\n"
    "ds_read_b64_tr_b16 v[86:87], v18 offset:6144\n"
    "v_cvt_pk_bf16_f32 v126, v60, v61\n"
    "v_mfma_f32_16x16x32_bf16 v[36:39], v[68:71], a[20:23], v[36:39]\n"
    "ds_read_b64_tr_b16 v[88:89], v18 offset:24576\n"
    "v_cvt_pk_bf16_f32 v127, v62, v63\n"
    "v_mfma_f32_16x16x32_bf16 v[36:39], v[72:75], a[36:39], v[36:39]\n"
    "buffer_load_dwordx4 v13, s[48:51], s73 offen offset:1024 lds\n"
    "v_mfma_f32_16x16x32_bf16 v[36:39], v[76:79], a[52:55], v[36:39]\n"
    "ds_read_b64_tr_b16 v[90:91], v18 offset:26624\n"
    "v_mfma_f32_16x16x32_bf16 v[40:43], v[64:67], a[8:11], 0\n"
    "ds_read_b64_tr_b16 v[92:93], v18 offset:28672\n"
    "v_mfma_f32_16x16x32_bf16 v[40:43], v[68:71], a[24:27], v[40:43]\n"
    "ds_read_b64_tr_b16 v[94:95], v18 offset:30720\n"
    "v_mfma_f32_16x16x32_bf16 v[40:43], v[72:75], a[40:43], v[40:43]\n"
    "buffer_load_dwordx4 v14, s[48:51], s73 offen offset:2048 lds\n"
    "v_mfma_f32_16x16x32_bf16 v[40:43], v[76:79], a[56:59], v[40:43]\n"
    "v_mfma_f32_16x16x32_bf16 v[44:47], v[64:67], a[12:15], 0\n"
    "v_mfma_f32_16x16x32_bf16 v[44:47], v[68:71], a[28:31], v[44:47]\n"
    "v_mfma_f32_16x16x32_bf16 v[44:47], v[72:75], a[44:47], v[44:47]\n"
    "buffer_load_dwordx4 v15, s[48:51], s73 offen offset:3072 lds\n"
    "v_mfma_f32_16x16x32_bf16 v[44:47], v[76:79], a[60:63], v[44:47]\n"
    "s_waitcnt vmcnt(8) lgkmcnt(0)\n"
    "s_mov_b64 exec, 3\n"
    "buffer_store_dword v24, v25, s[52:55], 0 offen sc1\n"
    "s_mov_b64 exec, -1\n"
    "s_barrier\n"
    "v_mfma_f32_16x16x32_bf16 v[32:35], v[80:83], a[64:67], v[32:35]\n"
    "ds_read_b64_tr_b16 v[64:65], v17 offset:8192\n"
    "v_mfma_f32_16x16x32_bf16 v[32:35], v[84:87], a[80:83], v[32:35]\n"
    "s_xor_b32 s72, s72, 262144\n"
    "s_add_u32 s73, s72, 32768\n"
    "s_add_u32 m0, s77, 32768\n"
    "ds_read_b64_tr_b16 v[66:67], v17 offset:10240\n"
    "v_mfma_f32_16x16x32_bf16 v[32:35], v[88:91], a[96:99], v[32:35]\n"
    "buffer_load_dwordx4 v12, s[48:51], s72 offen offset:0 lds\n"
    "v_mfma_f32_16x16x32_bf16 v[32:35], v[92:95], a[112:115], v[32:35]\n"
    "ds_read_b64_tr_b16 v[68:69], v17 offset:12288\n"
    "v_mfma_f32_16x16x32_bf16 v[36:39], v[80:83], a[68:71], v[36:39]\n"
    "ds_read_b64_tr_b16 v[70:71], v17 offset:14336\n"
    "v_mfma_f32_16x16x32_bf16 v[36:39], v[84:87], a[84:87], v[36:39]\n"
    "ds_read_b64_tr_b16 v[72:73], v17 offset:32768\n"
    "v_mfma_f32_16x16x32_bf16 v[36:39], v[88:91], a[100:103], v[36:39]\n"
    "buffer_load_dwordx4 v13, s[48:51], s72 offen offset:1024 lds\n"
    "v_mfma_f32_16x16x32_bf16 v[36:39], v[92:95], a[116:119], v[36:39]\n"
    "ds_read_b64_tr_b16 v[74:75], v17 offset:34816\n"
    "v_mfma_f32_16x16x32_bf16 v[40:43], v[80:83], a[72:75], v[40:43]\n"
    "ds_read_b64_tr_b16 v[76:77], v17 offset:36864\n"
    "v_mfma_f32_16x16x32_bf16 v[40:43], v[84:87], a[88:91], v[40:43]\n"
    "ds_read_b64_tr_b16 v[78:79], v17 offset:38912\n"
    "v_mfma_f32_16x16x32_bf16 v[40:43], v[88:91], a[104:107], v[40:43]\n"
    "buffer_load_dwordx4 v14, s[48:51], s72 offen offset:2048 lds\n"
    "v_mfma_f32_16x16x32_bf16 v[40:43], v[92:95], a[120:123], v[40:43]\n"
    "buffer_store_dwordx4 v[112:115], v1, s[52:55], s14 offen sc1\n"
    "v_mfma_f32_16x16x32_bf16 v[44:47], v[80:83], a[76:79], v[44:47]\n"
    "v_mfma_f32_16x16x32_bf16 v[44:47], v[84:87], a[92:95], v[44:47]\n"
    "v_mfma_f32_16x16x32_bf16 v[44:47], v[88:91], a[108:111], v[44:47]\n"
    "buffer_load_dwordx4 v15, s[48:51], s72 offen offset:3072 lds\n"
    "v_mfma_f32_16x16x32_bf16 v[44:47], v[92:95], a[124:127], v[44:47]\n"
    "s_waitcnt lgkmcnt(6)\n"
    "buffer_store_dwordx4 v[116:119], v1, s[52:55], s14 offen offset:1024 sc1\n"
    "v_mfma_f32_16x16x32_bf16 v[32:35], v[64:67], a[128:131], v[32:35]\n"
    "s_waitcnt lgkmcnt(4)\n"
    "ds_read_b64_tr_b16 v[80:81], v18 offset:8192\n"
    "v_mfma_f32_16x16x32_bf16 v[32:35], v[68:71], a[144:147], v[32:35]\n"
    "s_waitcnt lgkmcnt(3)\n"
    "s_add_u32 m0, s77, 36864\n"
    "ds_read_b64_tr_b16 v[82:83], v18 offset:10240\n"
    "v_mfma_f32_16x16x32_bf16 v[32:35], v[72:75], a[160:163], v[32:35]\n"
    "s_waitcnt lgkmcnt(2)\n"
    "buffer_load_dwordx4 v12, s[48:51], s73 offen offset:0 lds\n"
    "v_mfma_f32_16x16x32_bf16 v[32:35], v[76:79], a[176:179], v[32:35]\n"
    "ds_read_b64_tr_b16 v[84:85], v18 offset:12288\n"
    "v_mfma_f32_16x16x32_bf16 v[36:39], v[64:67], a[132:135], v[36:39]\n"
    "ds_read_b64_tr_b16 v[86:87], v18 offset:14336\n"
    "v_mfma_f32_16x16x32_bf16 v[36:39], v[68:71], a[148:151], v[36:39]\n"
    "ds_read_b64_tr_b16 v[88:89], v18 offset:32768\n"
    "v_mfma_f32_16x16x32_bf16 v[36:39], v[72:75], a[164:167], v[36:39]\n"
    "buffer_load_dwordx4 v13, s[48:51], s73 offen offset:1024 lds\n"
    "v_mfma_f32_16x16x32_bf16 v[36:39], v[76:79], a[180:183], v[36:39]\n"
    "ds_read_b64_tr_b16 v[90:91], v18 offset:34816\n"
    "v_mfma_f32_16x16x32_bf16 v[40:43], v[64:67], a[136:139], v[40:43]\n"
    "ds_read_b64_tr_b16 v[92:93], v18 offset:36864\n"
    "v_mfma_f32_16x16x32_bf16 v[40:43], v[68:71], a[152:155], v[40:43]\n"
    "ds_read_b64_tr_b16 v[94:95], v18 offset:38912\n"
    "v_mfma_f32_16x16x32_bf16 v[40:43], v[72:75], a[168:171], v[40:43]\n"
    "buffer_load_dwordx4 v14, s[48:51], s73 offen offset:2048 lds\n"
    "v_mfma_f32_16x16x32_bf16 v[40:43], v[76:79], a[184:187], v[40:43]\n"
    "buffer_store_dwordx4 v[120:123], v1, s[52:55], s15 offen sc1\n"
    "v_mfma_f32_16x16x32_bf16 v[44:47], v[64:67], a[140:143], v[44:47]\n"
    "v_mfma_f32_16x16x32_bf16 v[44:47], v[68:71], a[156:159], v[44:47]\n"
    "v_mfma_f32_16x16x32_bf16 v[44:47], v[72:75], a[172:175], v[44:47]\n"
    "buffer_load_dwordx4 v15, s[48:51], s73 offen offset:3072 lds\n"
    "v_mfma_f32_16x16x32_bf16 v[44:47], v[76:79], a[188:191], v[44:47]\n"
    "s_waitcnt vmcnt(12) lgkmcnt(0)\n"
    "buffer_store_dwordx4 v[124:127], v1, s[52:55], s15 offen offset:1024 sc1\n"
    "s_barrier\n"
    "v_mfma_f32_16x16x32_bf16 v[32:35], v[80:83], a[192:195], v[32:35]\n"
    "ds_read_b64_tr_b16 v[64:65], v16 offset:0\n"
    "v_mfma_f32_16x16x32_bf16 v[32:35], v[84:87], a[208:211], v[32:35]\n"
    "s_add_u32 s72, s27, 7\n"
    "s_and_b32 s72, s72, 7\n"
    "s_lshl_b32 s72, s72, 7\n"
    "s_add_u32 s72, s72, s28\n"
    "s_add_u32 s73, s72, 32768\n"
    "s_add_u32 m0, s66, 16384\n"
    "ds_read_b64_tr_b16 v[66:67], v16 offset:2048\n"
    "v_mfma_f32_16x16x32_bf16 v[32:35], v[88:91], a[224:227], v[32:35]\n"
    "buffer_load_dwordx4 v12, s[48:51], s72 offen offset:0 lds\n"
    "v_mfma_f32_16x16x32_bf16 v[32:35], v[92:95], a[240:243], v[32:35]\n"
    "ds_read_b64_tr_b16 v[68:69], v16 offset:4096\n"
    "v_mfma_f32_16x16x32_bf16 v[36:39], v[80:83], a[196:199], v[36:39]\n"
    "ds_read_b64_tr_b16 v[70:71], v16 offset:6144\n"
    "v_mfma_f32_16x16x32_bf16 v[36:39], v[84:87], a[212:215], v[36:39]\n"
    "ds_read_b64_tr_b16 v[72:73], v16 offset:8192\n"
    "v_mfma_f32_16x16x32_bf16 v[36:39], v[88:91], a[228:231], v[36:39]\n"
    "buffer_load_dwordx4 v13, s[48:51], s72 offen offset:1024 lds\n"
    "v_mfma_f32_16x16x32_bf16 v[36:39], v[92:95], a[244:247], v[36:39]\n"
    "ds_read_b64_tr_b16 v[74:75], v16 offset:10240\n"
    "v_mfma_f32_16x16x32_bf16 v[40:43], v[80:83], a[200:203], v[40:43]\n"
    "ds_read_b64_tr_b16 v[76:77], v16 offset:12288\n"
    "v_cvt_pk_bf16_f32 v128, v32, v33\n"
    "v_mfma_f32_16x16x32_bf16 v[40:43], v[84:87], a[216:219], v[40:43]\n"
    "ds_read_b64_tr_b16 v[78:79], v16 offset:14336\n"
    "v_cvt_pk_bf16_f32 v129, v34, v35\n"
    "v_mfma_f32_16x16x32_bf16 v[40:43], v[88:91], a[232:235], v[40:43]\n"
    "buffer_load_dwordx4 v14, s[48:51], s72 offen offset:2048 lds\n"
    "v_mfma_f32_16x16x32_bf16 v[40:43], v[92:95], a[248:251], v[40:43]\n"
    "v_mfma_f32_16x16x32_bf16 v[44:47], v[80:83], a[204:207], v[44:47]\n"
    "v_cvt_pk_bf16_f32 v130, v36, v37\n"
    "v_cvt_pk_bf16_f32 v131, v38, v39\n"
    "v_mfma_f32_16x16x32_bf16 v[44:47], v[84:87], a[220:223], v[44:47]\n"
    "v_mfma_f32_16x16x32_bf16 v[44:47], v[88:91], a[236:239], v[44:47]\n"
    "buffer_load_dwordx4 v15, s[48:51], s72 offen offset:3072 lds\n"
    "v_mfma_f32_16x16x32_bf16 v[44:47], v[92:95], a[252:255], v[44:47]\n"
    "s_waitcnt lgkmcnt(6)\n"
    "v_mfma_f32_16x16x32_bf16 v[48:51], v[64:67], a[0:3], 0\n"
    "s_waitcnt lgkmcnt(4)\n"
    "ds_read_b64_tr_b16 v[80:81], v16 offset:16384\n"
    "v_cvt_pk_bf16_f32 v132, v40, v41\n"
    "v_mfma_f32_16x16x32_bf16 v[48:51], v[68:71], a[16:19], v[48:51]\n"
    "s_waitcnt lgkmcnt(3)\n"
    "s_add_u32 m0, s66, 20480\n"
    "ds_read_b64_tr_b16 v[82:83], v16 offset:18432\n"
    "v_cvt_pk_bf16_f32 v133, v42, v43\n"
    "v_mfma_f32_16x16x32_bf16 v[48:51], v[72:75], a[32:35], v[48:51]\n"
    "s_waitcnt lgkmcnt(2)\n"
    "buffer_load_dwordx4 v12, s[48:51], s73 offen offset:0 lds\n"
    "v_mfma_f32_16x16x32_bf16 v[48:51], v[76:79], a[48:51], v[48:51]\n"
    "ds_read_b64_tr_b16 v[84:85], v16 offset:20480\n"
    "v_mfma_f32_16x16x32_bf16 v[52:55], v[64:67], a[4:7], 0\n"
    "ds_read_b64_tr_b16 v[86:87], v16 offset:22528\n"
    "v_cvt_pk_bf16_f32 v134, v44, v45\n"
    "v_mfma_f32_16x16x32_bf16 v[52:55], v[68:71], a[20:23], v[52:55]\n"
    "ds_read_b64_tr_b16 v[88:89], v16 offset:24576\n"
    "v_cvt_pk_bf16_f32 v135, v46, v47\n"
    "v_mfma_f32_16x16x32_bf16 v[52:55], v[72:75], a[36:39], v[52:55]\n"
    "buffer_load_dwordx4 v13, s[48:51], s73 offen offset:1024 lds\n"
    "v_mfma_f32_16x16x32_bf16 v[52:55], v[76:79], a[52:55], v[52:55]\n"
    "ds_read_b64_tr_b16 v[90:91], v16 offset:26624\n"
    "v_mfma_f32_16x16x32_bf16 v[56:59], v[64:67], a[8:11], 0\n"
    "ds_read_b64_tr_b16 v[92:93], v16 offset:28672\n"
    "v_mfma_f32_16x16x32_bf16 v[56:59], v[68:71], a[24:27], v[56:59]\n"
    "ds_read_b64_tr_b16 v[94:95], v16 offset:30720\n"
    "v_mfma_f32_16x16x32_bf16 v[56:59], v[72:75], a[40:43], v[56:59]\n"
    "buffer_load_dwordx4 v14, s[48:51], s73 offen offset:2048 lds\n"
    "v_mfma_f32_16x16x32_bf16 v[56:59], v[76:79], a[56:59], v[56:59]\n"
    "v_mfma_f32_16x16x32_bf16 v[60:63], v[64:67], a[12:15], 0\n"
    "v_mfma_f32_16x16x32_bf16 v[60:63], v[68:71], a[28:31], v[60:63]\n"
    "v_mfma_f32_16x16x32_bf16 v[60:63], v[72:75], a[44:47], v[60:63]\n"
    "buffer_load_dwordx4 v15, s[48:51], s73 offen offset:3072 lds\n"
    "v_mfma_f32_16x16x32_bf16 v[60:63], v[76:79], a[60:63], v[60:63]\n"
    "s_waitcnt vmcnt(9) lgkmcnt(0)\n"
    "s_barrier\n"
    "v_mfma_f32_16x16x32_bf16 v[48:51], v[80:83], a[64:67], v[48:51]\n"
    "ds_read_b64_tr_b16 v[64:65], v16 offset:32768\n"
    "v_mfma_f32_16x16x32_bf16 v[48:51], v[84:87], a[80:83], v[48:51]\n"
    "s_xor_b32 s72, s72, 262144\n"
    "s_add_u32 s73, s72, 32768\n"
    "s_add_u32 m0, s66, 0\n"
    "ds_read_b64_tr_b16 v[66:67], v16 offset:34816\n"
    "v_mfma_f32_16x16x32_bf16 v[48:51], v[88:91], a[96:99], v[48:51]\n"
    "buffer_load_dwordx4 v12, s[48:51], s72 offen offset:0 lds\n"
    "v_mfma_f32_16x16x32_bf16 v[48:51], v[92:95], a[112:115], v[48:51]\n"
    "ds_read_b64_tr_b16 v[68:69], v16 offset:36864\n"
    "v_mfma_f32_16x16x32_bf16 v[52:55], v[80:83], a[68:71], v[52:55]\n"
    "ds_read_b64_tr_b16 v[70:71], v16 offset:38912\n"
    "v_mfma_f32_16x16x32_bf16 v[52:55], v[84:87], a[84:87], v[52:55]\n"
    "ds_read_b64_tr_b16 v[72:73], v16 offset:40960\n"
    "v_mfma_f32_16x16x32_bf16 v[52:55], v[88:91], a[100:103], v[52:55]\n"
    "buffer_load_dwordx4 v13, s[48:51], s72 offen offset:1024 lds\n"
    "v_mfma_f32_16x16x32_bf16 v[52:55], v[92:95], a[116:119], v[52:55]\n"
    "ds_read_b64_tr_b16 v[74:75], v16 offset:43008\n"
    "v_mfma_f32_16x16x32_bf16 v[56:59], v[80:83], a[72:75], v[56:59]\n"
    "ds_read_b64_tr_b16 v[76:77], v16 offset:45056\n"
    "v_mfma_f32_16x16x32_bf16 v[56:59], v[84:87], a[88:91], v[56:59]\n"
    "ds_read_b64_tr_b16 v[78:79], v16 offset:47104\n"
    "v_mfma_f32_16x16x32_bf16 v[56:59], v[88:91], a[104:107], v[56:59]\n"
    "buffer_load_dwordx4 v14, s[48:51], s72 offen offset:2048 lds\n"
    "v_mfma_f32_16x16x32_bf16 v[56:59], v[92:95], a[120:123], v[56:59]\n"
    "v_mfma_f32_16x16x32_bf16 v[60:63], v[80:83], a[76:79], v[60:63]\n"
    "v_mfma_f32_16x16x32_bf16 v[60:63], v[84:87], a[92:95], v[60:63]\n"
    "v_mfma_f32_16x16x32_bf16 v[60:63], v[88:91], a[108:111], v[60:63]\n"
    "buffer_load_dwordx4 v15, s[48:51], s72 offen offset:3072 lds\n"
    "v_mfma_f32_16x16x32_bf16 v[60:63], v[92:95], a[124:127], v[60:63]\n"
    "s_waitcnt lgkmcnt(6)\n"
    "v_mfma_f32_16x16x32_bf16 v[48:51], v[64:67], a[128:131], v[48:51]\n"
    "s_waitcnt lgkmcnt(4)\n"
    "ds_read_b64_tr_b16 v[80:81], v16 offset:49152\n"
    "v_mfma_f32_16x16x32_bf16 v[48:51], v[68:71], a[144:147], v[48:51]\n"
    "s_waitcnt lgkmcnt(3)\n"
    "s_add_u32 m0, s66, 4096\n"
    "ds_read_b64_tr_b16 v[82:83], v16 offset:51200\n"
    "v_mfma_f32_16x16x32_bf16 v[48:51], v[72:75], a[160:163], v[48:51]\n"
    "s_waitcnt lgkmcnt(2)\n"
    "buffer_load_dwordx4 v12, s[48:51], s73 offen offset:0 lds\n"
    "v_mfma_f32_16x16x32_bf16 v[48:51], v[76:79], a[176:179], v[48:51]\n"
    "ds_read_b64_tr_b16 v[84:85], v16 offset:53248\n"
    "v_mfma_f32_16x16x32_bf16 v[52:55], v[64:67], a[132:135], v[52:55]\n"
    "ds_read_b64_tr_b16 v[86:87], v16 offset:55296\n"
    "v_mfma_f32_16x16x32_bf16 v[52:55], v[68:71], a[148:151], v[52:55]\n"
    "ds_read_b64_tr_b16 v[88:89], v16 offset:57344\n"
    "v_mfma_f32_16x16x32_bf16 v[52:55], v[72:75], a[164:167], v[52:55]\n"
    "buffer_load_dwordx4 v13, s[48:51], s73 offen offset:1024 lds\n"
    "v_mfma_f32_16x16x32_bf16 v[52:55], v[76:79], a[180:183], v[52:55]\n"
    "ds_read_b64_tr_b16 v[90:91], v16 offset:59392\n"
    "v_mfma_f32_16x16x32_bf16 v[56:59], v[64:67], a[136:139], v[56:59]\n"
    "ds_read_b64_tr_b16 v[92:93], v16 offset:61440\n"
    "v_mfma_f32_16x16x32_bf16 v[56:59], v[68:71], a[152:155], v[56:59]\n"
    "ds_read_b64_tr_b16 v[94:95], v16 offset:63488\n"
    "v_mfma_f32_16x16x32_bf16 v[56:59], v[72:75], a[168:171], v[56:59]\n"
    "buffer_load_dwordx4 v14, s[48:51], s73 offen offset:2048 lds\n"
    "v_mfma_f32_16x16x32_bf16 v[56:59], v[76:79], a[184:187], v[56:59]\n"
    "s_waitcnt vmcnt(15)\n"
    "s_mov_b64 exec, 12\n"
    "buffer_store_dword v24, v25, s[52:55], 0 offen sc1\n"
    "s_mov_b64 exec, -1\n"
    "s_nop 0\n"
    "v_mfma_f32_16x16x32_bf16 v[60:63], v[64:67], a[140:143], v[60:63]\n"
    "v_mfma_f32_16x16x32_bf16 v[60:63], v[68:71], a[156:159], v[60:63]\n"
    "v_mfma_f32_16x16x32_bf16 v[60:63], v[72:75], a[172:175], v[60:63]\n"
    "buffer_load_dwordx4 v15, s[48:51], s73 offen offset:3072 lds\n"
    "v_mfma_f32_16x16x32_bf16 v[60:63], v[76:79], a[188:191], v[60:63]\n"
    "s_waitcnt vmcnt(9) lgkmcnt(0)\n"
    "buffer_store_dwordx4 v[128:131], v1, s[52:55], s16 offen sc1\n"
    "s_barrier\n"
    "v_mfma_f32_16x16x32_bf16 v[48:51], v[80:83], a[192:195], v[48:51]\n"
    "ds_read_b64_tr_b16 v[64:65], v17 offset:16384\n"
    "v_mfma_f32_16x16x32_bf16 v[48:51], v[84:87], a[208:211], v[48:51]\n"
    "s_add_u32 s72, s27, 8\n"
    "s_and_b32 s72, s72, 7\n"
    "s_lshl_b32 s72, s72, 7\n"
    "s_add_u32 s72, s72, s28\n"
    "s_add_u32 s73, s72, 32768\n"
    "s_add_u32 m0, s66, 8192\n"
    "ds_read_b64_tr_b16 v[66:67], v17 offset:18432\n"
    "v_mfma_f32_16x16x32_bf16 v[48:51], v[88:91], a[224:227], v[48:51]\n"
    "buffer_load_dwordx4 v12, s[48:51], s72 offen offset:0 lds\n"
    "v_mfma_f32_16x16x32_bf16 v[48:51], v[92:95], a[240:243], v[48:51]\n"
    "ds_read_b64_tr_b16 v[68:69], v17 offset:20480\n"
    "v_mfma_f32_16x16x32_bf16 v[52:55], v[80:83], a[196:199], v[52:55]\n"
    "ds_read_b64_tr_b16 v[70:71], v17 offset:22528\n"
    "v_mfma_f32_16x16x32_bf16 v[52:55], v[84:87], a[212:215], v[52:55]\n"
    "ds_read_b64_tr_b16 v[72:73], v17 offset:40960\n"
    "v_mfma_f32_16x16x32_bf16 v[52:55], v[88:91], a[228:231], v[52:55]\n"
    "buffer_load_dwordx4 v13, s[48:51], s72 offen offset:1024 lds\n"
    "v_mfma_f32_16x16x32_bf16 v[52:55], v[92:95], a[244:247], v[52:55]\n"
    "ds_read_b64_tr_b16 v[74:75], v17 offset:43008\n"
    "v_mfma_f32_16x16x32_bf16 v[56:59], v[80:83], a[200:203], v[56:59]\n"
    "ds_read_b64_tr_b16 v[76:77], v17 offset:45056\n"
    "v_cvt_pk_bf16_f32 v136, v48, v49\n"
    "v_mfma_f32_16x16x32_bf16 v[56:59], v[84:87], a[216:219], v[56:59]\n"
    "ds_read_b64_tr_b16 v[78:79], v17 offset:47104\n"
    "v_cvt_pk_bf16_f32 v137, v50, v51\n"
    "v_mfma_f32_16x16x32_bf16 v[56:59], v[88:91], a[232:235], v[56:59]\n"
    "buffer_load_dwordx4 v14, s[48:51], s72 offen offset:2048 lds\n"
    "v_mfma_f32_16x16x32_bf16 v[56:59], v[92:95], a[248:251], v[56:59]\n"
    "buffer_store_dwordx4 v[132:135], v1, s[52:55], s16 offen offset:1024 sc1\n"
    "v_mfma_f32_16x16x32_bf16 v[60:63], v[80:83], a[204:207], v[60:63]\n"
    "v_cvt_pk_bf16_f32 v138, v52, v53\n"
    "v_cvt_pk_bf16_f32 v139, v54, v55\n"
    "v_mfma_f32_16x16x32_bf16 v[60:63], v[84:87], a[220:223], v[60:63]\n"
    "v_mfma_f32_16x16x32_bf16 v[60:63], v[88:91], a[236:239], v[60:63]\n"
    "buffer_load_dwordx4 v15, s[48:51], s72 offen offset:3072 lds\n"
    "v_mfma_f32_16x16x32_bf16 v[60:63], v[92:95], a[252:255], v[60:63]\n"
    "s_waitcnt lgkmcnt(6)\n"
    "buffer_load_dword v23, v21, s[52:55], 0 offen sc1\n"
    "v_mfma_f32_16x16x32_bf16 v[32:35], v[64:67], a[0:3], 0\n"
    "s_waitcnt lgkmcnt(4)\n"
    "ds_read_b64_tr_b16 v[80:81], v18 offset:16384\n"
    "v_cvt_pk_bf16_f32 v140, v56, v57\n"
    "v_mfma_f32_16x16x32_bf16 v[32:35], v[68:71], a[16:19], v[32:35]\n"
    "s_waitcnt lgkmcnt(3)\n"
    "s_add_u32 m0, s66, 12288\n"
    "ds_read_b64_tr_b16 v[82:83], v18 offset:18432\n"
    "v_cvt_pk_bf16_f32 v141, v58, v59\n"
    "v_mfma_f32_16x16x32_bf16 v[32:35], v[72:75], a[32:35], v[32:35]\n"
    "s_waitcnt lgkmcnt(2)\n"
    "buffer_load_dwordx4 v12, s[48:51], s73 offen offset:0 lds\n"
    "v_mfma_f32_16x16x32_bf16 v[32:35], v[76:79], a[48:51], v[32:35]\n"
    "ds_read_b64_tr_b16 v[84:85], v18 offset:20480\n"
    "v_mfma_f32_16x16x32_bf16 v[36:39], v[64:67], a[4:7], 0\n"
    "ds_read_b64_tr_b16 v[86:87], v18 offset:22528\n"
    "v_cvt_pk_bf16_f32 v142, v60, v61\n"
    "v_mfma_f32_16x16x32_bf16 v[36:39], v[68:71], a[20:23], v[36:39]\n"
    "ds_read_b64_tr_b16 v[88:89], v18 offset:40960\n"
    "v_cvt_pk_bf16_f32 v143, v62, v63\n"
    "v_mfma_f32_16x16x32_bf16 v[36:39], v[72:75], a[36:39], v[36:39]\n"
    "buffer_load_dwordx4 v13, s[48:51], s73 offen offset:1024 lds\n"
    "v_mfma_f32_16x16x32_bf16 v[36:39], v[76:79], a[52:55], v[36:39]\n"
    "ds_read_b64_tr_b16 v[90:91], v18 offset:43008\n"
    "v_mfma_f32_16x16x32_bf16 v[40:43], v[64:67], a[8:11], 0\n"
    "ds_read_b64_tr_b16 v[92:93], v18 offset:45056\n"
    "v_mfma_f32_16x16x32_bf16 v[40:43], v[68:71], a[24:27], v[40:43]\n"
    "ds_read_b64_tr_b16 v[94:95], v18 offset:47104\n"
    "v_mfma_f32_16x16x32_bf16 v[40:43], v[72:75], a[40:43], v[40:43]\n"
    "buffer_load_dwordx4 v14, s[48:51], s73 offen offset:2048 lds\n"
    "v_mfma_f32_16x16x32_bf16 v[40:43], v[76:79], a[56:59], v[40:43]\n"
    "v_mfma_f32_16x16x32_bf16 v[44:47], v[64:67], a[12:15], 0\n"
    "v_mfma_f32_16x16x32_bf16 v[44:47], v[68:71], a[28:31], v[44:47]\n"
    "v_mfma_f32_16x16x32_bf16 v[44:47], v[72:75], a[44:47], v[44:47]\n"
    "buffer_load_dwordx4 v15, s[48:51], s73 offen offset:3072 lds\n"
    "v_mfma_f32_16x16x32_bf16 v[44:47], v[76:79], a[60:63], v[44:47]\n"
    "s_waitcnt vmcnt(11) lgkmcnt(0)\n"
    "s_barrier\n"
    "v_mfma_f32_16x16x32_bf16 v[32:35], v[80:83], a[64:67], v[32:35]\n"
    "ds_read_b64_tr_b16 v[64:65], v17 offset:0\n"
    "v_mfma_f32_16x16x32_bf16 v[32:35], v[84:87], a[80:83], v[32:35]\n"
    "s_xor_b32 s72, s72, 262144\n"
    "s_add_u32 s73, s72, 32768\n"
    "s_add_u32 m0, s77, 0\n"
    "ds_read_b64_tr_b16 v[66:67], v17 offset:2048\n"
    "v_mfma_f32_16x16x32_bf16 v[32:35], v[88:91], a[96:99], v[32:35]\n"
    "buffer_load_dwordx4 v12, s[48:51], s72 offen offset:0 lds\n"
    "v_mfma_f32_16x16x32_bf16 v[32:35], v[92:95], a[112:115], v[32:35]\n"
    "ds_read_b64_tr_b16 v[68:69], v17 offset:4096\n"
    "v_mfma_f32_16x16x32_bf16 v[36:39], v[80:83], a[68:71], v[36:39]\n"
    "ds_read_b64_tr_b16 v[70:71], v17 offset:6144\n"
    "v_mfma_f32_16x16x32_bf16 v[36:39], v[84:87], a[84:87], v[36:39]\n"
    "ds_read_b64_tr_b16 v[72:73], v17 offset:24576\n"
    "v_mfma_f32_16x16x32_bf16 v[36:39], v[88:91], a[100:103], v[36:39]\n"
    "buffer_load_dwordx4 v13, s[48:51], s72 offen offset:1024 lds\n"
    "v_mfma_f32_16x16x32_bf16 v[36:39], v[92:95], a[116:119], v[36:39]\n"
    "ds_read_b64_tr_b16 v[74:75], v17 offset:26624\n"
    "v_mfma_f32_16x16x32_bf16 v[40:43], v[80:83], a[72:75], v[40:43]\n"
    "ds_read_b64_tr_b16 v[76:77], v17 offset:28672\n"
    "v_mfma_f32_16x16x32_bf16 v[40:43], v[84:87], a[88:91], v[40:43]\n"
    "ds_read_b64_tr_b16 v[78:79], v17 offset:30720\n"
    "v_mfma_f32_16x16x32_bf16 v[40:43], v[88:91], a[104:107], v[40:43]\n"
    "buffer_load_dwordx4 v14, s[48:51], s72 offen offset:2048 lds\n"
    "v_mfma_f32_16x16x32_bf16 v[40:43], v[92:95], a[120:123], v[40:43]\n"
    "v_mfma_f32_16x16x32_bf16 v[44:47], v[80:83], a[76:79], v[44:47]\n"
    "v_mfma_f32_16x16x32_bf16 v[44:47], v[84:87], a[92:95], v[44:47]\n"
    "v_mfma_f32_16x16x32_bf16 v[44:47], v[88:91], a[108:111], v[44:47]\n"
    "buffer_load_dwordx4 v15, s[48:51], s72 offen offset:3072 lds\n"
    "v_mfma_f32_16x16x32_bf16 v[44:47], v[92:95], a[124:127], v[44:47]\n"
    "s_waitcnt lgkmcnt(6)\n"
    "v_mfma_f32_16x16x32_bf16 v[32:35], v[64:67], a[128:131], v[32:35]\n"
    "s_waitcnt lgkmcnt(4)\n"
    "ds_read_b64_tr_b16 v[80:81], v18 offset:0\n"
    "v_mfma_f32_16x16x32_bf16 v[32:35], v[68:71], a[144:147], v[32:35]\n"
    "s_waitcnt lgkmcnt(3)\n"
    "s_add_u32 m0, s77, 4096\n"
    "ds_read_b64_tr_b16 v[82:83], v18 offset:2048\n"
    "v_mfma_f32_16x16x32_bf16 v[32:35], v[72:75], a[160:163], v[32:35]\n"
    "s_waitcnt lgkmcnt(2)\n"
    "buffer_load_dwordx4 v12, s[48:51], s73 offen offset:0 lds\n"
    "v_mfma_f32_16x16x32_bf16 v[32:35], v[76:79], a[176:179], v[32:35]\n"
    "ds_read_b64_tr_b16 v[84:85], v18 offset:4096\n"
    "v_mfma_f32_16x16x32_bf16 v[36:39], v[64:67], a[132:135], v[36:39]\n"
    "ds_read_b64_tr_b16 v[86:87], v18 offset:6144\n"
    "v_mfma_f32_16x16x32_bf16 v[36:39], v[68:71], a[148:151], v[36:39]\n"
    "ds_read_b64_tr_b16 v[88:89], v18 offset:24576\n"
    "v_mfma_f32_16x16x32_bf16 v[36:39], v[72:75], a[164:167], v[36:39]\n"
    "buffer_load_dwordx4 v13, s[48:51], s73 offen offset:1024 lds\n"
    "v_mfma_f32_16x16x32_bf16 v[36:39], v[76:79], a[180:183], v[36:39]\n"
    "ds_read_b64_tr_b16 v[90:91], v18 offset:26624\n"
    "v_mfma_f32_16x16x32_bf16 v[40:43], v[64:67], a[136:139], v[40:43]\n"
    "ds_read_b64_tr_b16 v[92:93], v18 offset:28672\n"
    "v_mfma_f32_16x16x32_bf16 v[40:43], v[68:71], a[152:155], v[40:43]\n"
    "ds_read_b64_tr_b16 v[94:95], v18 offset:30720\n"
    "v_mfma_f32_16x16x32_bf16 v[40:43], v[72:75], a[168:171], v[40:43]\n"
    "buffer_load_dwordx4 v14, s[48:51], s73 offen offset:2048 lds\n"
    "v_mfma_f32_16x16x32_bf16 v[40:43], v[76:79], a[184:187], v[40:43]\n"
    "v_mfma_f32_16x16x32_bf16 v[44:47], v[64:67], a[140:143], v[44:47]\n"
    "v_mfma_f32_16x16x32_bf16 v[44:47], v[68:71], a[156:159], v[44:47]\n"
    "v_mfma_f32_16x16x32_bf16 v[44:47], v[72:75], a[172:175], v[44:47]\n"
    "buffer_load_dwordx4 v15, s[48:51], s73 offen offset:3072 lds\n"
    "v_mfma_f32_16x16x32_bf16 v[44:47], v[76:79], a[188:191], v[44:47]\n"
    "s_waitcnt vmcnt(8) lgkmcnt(0)\n"
    "s_barrier\n"
    "v_mfma_f32_16x16x32_bf16 v[32:35], v[80:83], a[192:195], v[32:35]\n"
    "ds_read_b64_tr_b16 v[64:65], v17 offset:8192\n"
    "v_mfma_f32_16x16x32_bf16 v[32:35], v[84:87], a[208:211], v[32:35]\n"
    "ds_read_b64_tr_b16 v[66:67], v17 offset:10240\n"
    "v_mfma_f32_16x16x32_bf16 v[32:35], v[88:91], a[224:227], v[32:35]\n"
    "buffer_store_dwordx4 v[136:139], v1, s[52:55], s17 offen sc1\n"
    "v_mfma_f32_16x16x32_bf16 v[32:35], v[92:95], a[240:243], v[32:35]\n"
    "ds_read_b64_tr_b16 v[68:69], v17 offset:12288\n"
    "v_mfma_f32_16x16x32_bf16 v[36:39], v[80:83], a[196:199], v[36:39]\n"
    "ds_read_b64_tr_b16 v[70:71], v17 offset:14336\n"
    "v_mfma_f32_16x16x32_bf16 v[36:39], v[84:87], a[212:215], v[36:39]\n"
    "ds_read_b64_tr_b16 v[72:73], v17 offset:32768\n"
    "v_mfma_f32_16x16x32_bf16 v[36:39], v[88:91], a[228:231], v[36:39]\n"
    "buffer_store_dwordx4 v[140:143], v1, s[52:55], s17 offen offset:1024 sc1\n"
    "v_mfma_f32_16x16x32_bf16 v[36:39], v[92:95], a[244:247], v[36:39]\n"
    "ds_read_b64_tr_b16 v[74:75], v17 offset:34816\n"
    "v_mfma_f32_16x16x32_bf16 v[40:43], v[80:83], a[200:203], v[40:43]\n"
    "ds_read_b64_tr_b16 v[76:77], v17 offset:36864\n"
    "v_cvt_pk_bf16_f32 v144, v32, v33\n"
    "v_mfma_f32_16x16x32_bf16 v[40:43], v[84:87], a[216:219], v[40:43]\n"
    "ds_read_b64_tr_b16 v[78:79], v17 offset:38912\n"
    "v_cvt_pk_bf16_f32 v145, v34, v35\n"
    "v_mfma_f32_16x16x32_bf16 v[40:43], v[88:91], a[232:235], v[40:43]\n"
    "v_cmp_eq_u32 vcc, s80, v23\n"
    "s_nop 1\n"
    "s_and_b32 s31, vcc_lo, 0x7f\n"
    "s_or_b32 s88, s88, s83\n"
    "s_andn2_b32 s82, s31, s88\n"
    "s_and_b32 s82, s82, 3\n"
    "s_or_b32 s88, s88, s82\n"
    "s_or_b32 s30, s30, s82\n"
    "s_bitcmp1_b32 s82, 0\n"
    "s_cbranch_scc0 L_d12_%=\n"
    "buffer_load_dwordx4 v[152:155], v1, s[52:55], s4 offen sc1\n"
    "buffer_load_dwordx4 v[156:159], v1, s[52:55], s4 offen offset:1024 sc1\n"
    "L_d12_%=:\n"
    "v_mfma_f32_16x16x32_bf16 v[40:43], v[92:95], a[248:251], v[40:43]\n"
    "s_bitcmp1_b32 s82, 1\n"
    "s_cbranch_scc0 L_d13_%=\n"
    "buffer_load_dwordx4 v[160:163], v1, s[52:55], s5 offen sc1\n"
    "buffer_load_dwordx4 v[164:167], v1, s[52:55], s5 offen offset:1024 sc1\n"
    "L_d13_%=:\n"
    "v_mfma_f32_16x16x32_bf16 v[44:47], v[80:83], a[204:207], v[44:47]\n"
    "v_cvt_pk_bf16_f32 v146, v36, v37\n"
    "v_cvt_pk_bf16_f32 v147, v38, v39\n"
    "v_mfma_f32_16x16x32_bf16 v[44:47], v[84:87], a[220:223], v[44:47]\n"
    "v_mfma_f32_16x16x32_bf16 v[44:47], v[88:91], a[236:239], v[44:47]\n"
    "v_mfma_f32_16x16x32_bf16 v[44:47], v[92:95], a[252:255], v[44:47]\n"
    "s_waitcnt lgkmcnt(6)\n"
    "v_mfma_f32_16x16x32_bf16 v[48:51], v[64:67], a[0:3], 0\n"
    "s_waitcnt lgkmcnt(4)\n"
    "ds_read_b64_tr_b16 v[80:81], v18 offset:8192\n"
    "v_cvt_pk_bf16_f32 v148, v40, v41\n"
    "v_mfma_f32_16x16x32_bf16 v[48:51], v[68:71], a[16:19], v[48:51]\n"
    "s_waitcnt lgkmcnt(3)\n"
    "ds_read_b64_tr_b16 v[82:83], v18 offset:10240\n"
    "v_cvt_pk_bf16_f32 v149, v42, v43\n"
    "v_mfma_f32_16x16x32_bf16 v[48:51], v[72:75], a[32:35], v[48:51]\n"
    "s_waitcnt lgkmcnt(2)\n"
    "v_mfma_f32_16x16x32_bf16 v[48:51], v[76:79], a[48:51], v[48:51]\n"
    "ds_read_b64_tr_b16 v[84:85], v18 offset:12288\n"
    "v_mfma_f32_16x16x32_bf16 v[52:55], v[64:67], a[4:7], 0\n"
    "ds_read_b64_tr_b16 v[86:87], v18 offset:14336\n"
    "v_cvt_pk_bf16_f32 v150, v44, v45\n"
    "v_mfma_f32_16x16x32_bf16 v[52:55], v[68:71], a[20:23], v[52:55]\n"
    "ds_read_b64_tr_b16 v[88:89], v18 offset:32768\n"
    "v_cvt_pk_bf16_f32 v151, v46, v47\n"
    "v_mfma_f32_16x16x32_bf16 v[52:55], v[72:75], a[36:39], v[52:55]\n"
    "v_mfma_f32_16x16x32_bf16 v[52:55], v[76:79], a[52:55], v[52:55]\n"
    "ds_read_b64_tr_b16 v[90:91], v18 offset:34816\n"
    "v_mfma_f32_16x16x32_bf16 v[56:59], v[64:67], a[8:11], 0\n"
    "ds_read_b64_tr_b16 v[92:93], v18 offset:36864\n"
    "v_mfma_f32_16x16x32_bf16 v[56:59], v[68:71], a[24:27], v[56:59]\n"
    "ds_read_b64_tr_b16 v[94:95], v18 offset:38912\n"
    "v_mfma_f32_16x16x32_bf16 v[56:59], v[72:75], a[40:43], v[56:59]\n"
    "v_mfma_f32_16x16x32_bf16 v[56:59], v[76:79], a[56:59], v[56:59]\n"
    "v_mfma_f32_16x16x32_bf16 v[60:63], v[64:67], a[12:15], 0\n"
    "v_mfma_f32_16x16x32_bf16 v[60:63], v[68:71], a[28:31], v[60:63]\n"
    "v_mfma_f32_16x16x32_bf16 v[60:63], v[72:75], a[44:47], v[60:63]\n"
    "buffer_store_dwordx4 v[144:147], v1, s[52:55], s18 offen sc1\n"
    "v_mfma_f32_16x16x32_bf16 v[60:63], v[76:79], a[60:63], v[60:63]\n"
    "s_waitcnt vmcnt(3) lgkmcnt(0)\n"
    "buffer_store_dwordx4 v[148:151], v1, s[52:55], s18 offen offset:1024 sc1\n"
    "s_barrier\n"
    "v_mfma_f32_16x16x32_bf16 v[48:51], v[80:83], a[64:67], v[48:51]\n"
    "ds_read_b64_tr_b16 v[64:65], v16 offset:0\n"
    "v_mfma_f32_16x16x32_bf16 v[48:51], v[84:87], a[80:83], v[48:51]\n"
    "ds_read_b64_tr_b16 v[66:67], v16 offset:2048\n"
    "v_mfma_f32_16x16x32_bf16 v[48:51], v[88:91], a[96:99], v[48:51]\n"
    "v_mfma_f32_16x16x32_bf16 v[48:51], v[92:95], a[112:115], v[48:51]\n"
    "ds_read_b64_tr_b16 v[68:69], v16 offset:4096\n"
    "v_mfma_f32_16x16x32_bf16 v[52:55], v[80:83], a[68:71], v[52:55]\n"
    "ds_read_b64_tr_b16 v[70:71], v16 offset:6144\n"
    "v_mfma_f32_16x16x32_bf16 v[52:55], v[84:87], a[84:87], v[52:55]\n"
    "ds_read_b64_tr_b16 v[72:73], v16 offset:8192\n"
    "v_mfma_f32_16x16x32_bf16 v[52:55], v[88:91], a[100:103], v[52:55]\n"
    "v_mfma_f32_16x16x32_bf16 v[52:55], v[92:95], a[116:119], v[52:55]\n"
    "ds_read_b64_tr_b16 v[74:75], v16 offset:10240\n"
    "v_mfma_f32_16x16x32_bf16 v[56:59], v[80:83], a[72:75], v[56:59]\n"
    "ds_read_b64_tr_b16 v[76:77], v16 offset:12288\n"
    "v_mfma_f32_16x16x32_bf16 v[56:59], v[84:87], a[88:91], v[56:59]\n"
    "ds_read_b64_tr_b16 v[78:79], v16 offset:14336\n"
    "v_mfma_f32_16x16x32_bf16 v[56:59], v[88:91], a[104:107], v[56:59]\n"
    "v_mfma_f32_16x16x32_bf16 v[56:59], v[92:95], a[120:123], v[56:59]\n"
    "v_mfma_f32_16x16x32_bf16 v[60:63], v[80:83], a[76:79], v[60:63]\n"
    "v_mfma_f32_16x16x32_bf16 v[60:63], v[84:87], a[92:95], v[60:63]\n"
    "v_mfma_f32_16x16x32_bf16 v[60:63], v[88:91], a[108:111], v[60:63]\n"
    "v_mfma_f32_16x16x32_bf16 v[60:63], v[92:95], a[124:127], v[60:63]\n"
    "s_waitcnt lgkmcnt(6)\n"
    "v_mfma_f32_16x16x32_bf16 v[48:51], v[64:67], a[128:131], v[48:51]\n"
    "s_waitcnt lgkmcnt(4)\n"
    "ds_read_b64_tr_b16 v[80:81], v16 offset:16384\n"
    "v_mfma_f32_16x16x32_bf16 v[48:51], v[68:71], a[144:147], v[48:51]\n"
    "s_waitcnt lgkmcnt(3)\n"
    "ds_read_b64_tr_b16 v[82:83], v16 offset:18432\n"
    "v_mfma_f32_16x16x32_bf16 v[48:51], v[72:75], a[160:163], v[48:51]\n"
    "s_waitcnt lgkmcnt(2)\n"
    "v_mfma_f32_16x16x32_bf16 v[48:51], v[76:79], a[176:179], v[48:51]\n"
    "ds_read_b64_tr_b16 v[84:85], v16 offset:20480\n"
    "v_mfma_f32_16x16x32_bf16 v[52:55], v[64:67], a[132:135], v[52:55]\n"
    "ds_read_b64_tr_b16 v[86:87], v16 offset:22528\n"
    "v_mfma_f32_16x16x32_bf16 v[52:55], v[68:71], a[148:151], v[52:55]\n"
    "ds_read_b64_tr_b16 v[88:89], v16 offset:24576\n"
    "v_mfma_f32_16x16x32_bf16 v[52:55], v[72:75], a[164:167], v[52:55]\n"
    "buffer_load_dword v23, v21, s[52:55], 0 offen sc1\n"
    "v_mfma_f32_16x16x32_bf16 v[52:55], v[76:79], a[180:183], v[52:55]\n"
    "ds_read_b64_tr_b16 v[90:91], v16 offset:26624\n"
    "v_mfma_f32_16x16x32_bf16 v[56:59], v[64:67], a[136:139], v[56:59]\n"
    "ds_read_b64_tr_b16 v[92:93], v16 offset:28672\n"
    "v_mfma_f32_16x16x32_bf16 v[56:59], v[68:71], a[152:155], v[56:59]\n"
    "ds_read_b64_tr_b16 v[94:95], v16 offset:30720\n"
    "v_mfma_f32_16x16x32_bf16 v[56:59], v[72:75], a[168:171], v[56:59]\n"
    "v_mfma_f32_16x16x32_bf16 v[56:59], v[76:79], a[184:187], v[56:59]\n"
    "v_mfma_f32_16x16x32_bf16 v[60:63], v[64:67], a[140:143], v[60:63]\n"
    "v_mfma_f32_16x16x32_bf16 v[60:63], v[68:71], a[156:159], v[60:63]\n"
    "v_mfma_f32_16x16x32_bf16 v[60:63], v[72:75], a[172:175], v[60:63]\n"
    "v_mfma_f32_16x16x32_bf16 v[60:63], v[76:79], a[188:191], v[60:63]\n"
    "s_waitcnt lgkmcnt(6)\n"
    "v_mfma_f32_16x16x32_bf16 v[48:51], v[80:83], a[192:195], v[48:51]\n"
    "s_waitcnt lgkmcnt(4)\n"
    "v_mfma_f32_16x16x32_bf16 v[48:51], v[84:87], a[208:211], v[48:51]\n"
    "s_waitcnt lgkmcnt(2)\n"
    "v_mfma_f32_16x16x32_bf16 v[48:51], v[88:91], a[224:227], v[48:51]\n"
    "s_waitcnt lgkmcnt(0)\n"
    "v_mfma_f32_16x16x32_bf16 v[48:51], v[92:95], a[240:243], v[48:51]\n"
    "v_mfma_f32_16x16x32_bf16 v[52:55], v[80:83], a[196:199], v[52:55]\n"
    "v_mfma_f32_16x16x32_bf16 v[52:55], v[84:87], a[212:215], v[52:55]\n"
    "v_mfma_f32_16x16x32_bf16 v[52:55], v[88:91], a[228:231], v[52:55]\n"
    "v_mfma_f32_16x16x32_bf16 v[52:55], v[92:95], a[244:247], v[52:55]\n"
    "v_mfma_f32_16x16x32_bf16 v[56:59], v[80:83], a[200:203], v[56:59]\n"
    "v_mfma_f32_16x16x32_bf16 v[56:59], v[84:87], a[216:219], v[56:59]\n"
    "v_mfma_f32_16x16x32_bf16 v[56:59], v[88:91], a[232:235], v[56:59]\n"
    "v_mfma_f32_16x16x32_bf16 v[56:59], v[92:95], a[248:251], v[56:59]\n"
    "v_mfma_f32_16x16x32_bf16 v[60:63], v[80:83], a[204:207], v[60:63]\n"
    "v_mfma_f32_16x16x32_bf16 v[60:63], v[84:87], a[220:223], v[60:63]\n"
    "v_mfma_f32_16x16x32_bf16 v[60:63], v[88:91], a[236:239], v[60:63]\n"
    "v_mfma_f32_16x16x32_bf16 v[60:63], v[92:95], a[252:255], v[60:63]\n"
    "s_waitcnt vmcnt(1)\n"
    "s_mov_b64 exec, 112\n"
    "buffer_store_dword v24, v25, s[52:55], 0 offen sc1\n"
    "s_mov_b64 exec, -1\n"
    "s_waitcnt vmcnt(1)\n"
    "v_cmp_eq_u32 vcc, s80, v23\n"
    "s_nop 1\n"
    "s_and_b32 s31, vcc_lo, 0x7f\n"
    "s_or_b32 s88, s88, s83\n"
    "s_andn2_b32 s82, s31, s88\n"
    "s_and_b32 s82, s82, 127\n"
    "s_or_b32 s88, s88, s82\n"
    "s_or_b32 s30, s30, s82\n"
    "s_bitcmp1_b32 s82, 0\n"
    "s_cbranch_scc0 L_d14_%=\n"
    "buffer_load_dwordx4 v[152:155], v1, s[52:55], s4 offen sc1\n"
    "buffer_load_dwordx4 v[156:159], v1, s[52:55], s4 offen offset:1024 sc1\n"
    "L_d14_%=:\n"
    "s_bitcmp1_b32 s82, 1\n"
    "s_cbranch_scc0 L_d15_%=\n"
    "buffer_load_dwordx4 v[160:163], v1, s[52:55], s5 offen sc1\n"
    "buffer_load_dwordx4 v[164:167], v1, s[52:55], s5 offen offset:1024 sc1\n"
    "L_d15_%=:\n"
    "s_bitcmp1_b32 s82, 2\n"
    "s_cbranch_scc0 L_d16_%=\n"
    "buffer_load_dwordx4 v[168:171], v1, s[52:55], s6 offen sc1\n"
    "buffer_load_dwordx4 v[172:175], v1, s[52:55], s6 offen offset:1024 sc1\n"
    "L_d16_%=:\n"
    "s_bitcmp1_b32 s82, 3\n"
    "s_cbranch_scc0 L_d17_%=\n"
    "buffer_load_dwordx4 v[176:179], v1, s[52:55], s7 offen sc1\n"
    "buffer_load_dwordx4 v[180:183], v1, s[52:55], s7 offen offset:1024 sc1\n"
    "L_d17_%=:\n"
    "s_bitcmp1_b32 s82, 4\n"
    "s_cbranch_scc0 L_d18_%=\n"
    "buffer_load_dwordx4 v[184:187], v1, s[52:55], s8 offen sc1\n"
    "buffer_load_dwordx4 v[188:191], v1, s[52:55], s8 offen offset:1024 sc1\n"
    "L_d18_%=:\n"
    "s_bitcmp1_b32 s82, 5\n"
    "s_cbranch_scc0 L_d19_%=\n"
    "buffer_load_dwordx4 v[192:195], v1, s[52:55], s9 offen sc1\n"
    "buffer_load_dwordx4 v[196:199], v1, s[52:55], s9 offen offset:1024 sc1\n"
    "L_d19_%=:\n"
    "s_bitcmp1_b32 s82, 6\n"
    "s_cbranch_scc0 L_d20_%=\n"
    "buffer_load_dwordx4 v[200:203], v1, s[52:55], s10 offen sc1\n"
    "buffer_load_dwordx4 v[204:207], v1, s[52:55], s10 offen offset:1024 sc1\n"
    "L_d20_%=:\n"
    "s_mov_b32 s91, 0\n"
    "s_or_b32 s88, s88, s83\n"
    "L_poll_%=:\n"
    "buffer_load_dword v23, v21, s[52:55], 0 offen sc1\n"
    "buffer_load_dword v28, v22, s[52:55], 0 offen sc1\n"
    "s_waitcnt vmcnt(0)\n"
    "v_cmp_ne_u32 vcc, v28, v24\n"
    "s_nop 1\n"
    "s_cbranch_vccz L_same_%=\n"
    "v_mov_b32 v24, v28\n"
    "s_mov_b32 exec_lo, s29\n"
    "s_mov_b32 exec_hi, 0\n"
    "buffer_store_dword v24, v25, s[52:55], 0 offen sc1\n"
    "s_mov_b64 exec, -1\n"
    "L_same_%=:\n"
    "v_cmp_eq_u32 vcc, s80, v23\n"
    "s_add_u32 s91, s91, 1\n"
    "s_nop 0\n"
    "s_and_b32 s82, vcc_lo, 0x7f\n"
    "s_andn2_b32 s30, s82, s88\n"
    "s_or_b32 s88, s88, s30\n"
    "s_bitcmp1_b32 s30, 0\n"
    "s_cbranch_scc0 L_nl0_%=\n"
    "buffer_load_dwordx4 v[152:155], v1, s[52:55], s4 offen sc1\n"
    "buffer_load_dwordx4 v[156:159], v1, s[52:55], s4 offen offset:1024 sc1\n"
    "L_nl0_%=:\n"
    "s_bitcmp1_b32 s30, 1\n"
    "s_cbranch_scc0 L_nl1_%=\n"
    "buffer_load_dwordx4 v[160:163], v1, s[52:55], s5 offen sc1\n"
    "buffer_load_dwordx4 v[164:167], v1, s[52:55], s5 offen offset:1024 sc1\n"
    "L_nl1_%=:\n"
    "s_bitcmp1_b32 s30, 2\n"
    "s_cbranch_scc0 L_nl2_%=\n"
    "buffer_load_dwordx4 v[168:171], v1, s[52:55], s6 offen sc1\n"
    "buffer_load_dwordx4 v[172:175], v1, s[52:55], s6 offen offset:1024 sc1\n"
    "L_nl2_%=:\n"
    "s_bitcmp1_b32 s30, 3\n"
    "s_cbranch_scc0 L_nl3_%=\n"
    "buffer_load_dwordx4 v[176:179], v1, s[52:55], s7 offen sc1\n"
    "buffer_load_dwordx4 v[180:183], v1, s[52:55], s7 offen offset:1024 sc1\n"
    "L_nl3_%=:\n"
    "s_bitcmp1_b32 s30, 4\n"
    "s_cbranch_scc0 L_nl4_%=\n"
    "buffer_load_dwordx4 v[184:187], v1, s[52:55], s8 offen sc1\n"
    "buffer_load_dwordx4 v[188:191], v1, s[52:55], s8 offen offset:1024 sc1\n"
    "L_nl4_%=:\n"
    "s_bitcmp1_b32 s30, 5\n"
    "s_cbranch_scc0 L_nl5_%=\n"
    "buffer_load_dwordx4 v[192:195], v1, s[52:55], s9 offen sc1\n"
    "buffer_load_dwordx4 v[196:199], v1, s[52:55], s9 offen offset:1024 sc1\n"
    "L_nl5_%=:\n"
    "s_bitcmp1_b32 s30, 6\n"
    "s_cbranch_scc0 L_nl6_%=\n"
    "buffer_load_dwordx4 v[200:203], v1, s[52:55], s10 offen sc1\n"
    "buffer_load_dwordx4 v[204:207], v1, s[52:55], s10 offen offset:1024 sc1\n"
    "L_nl6_%=:\n"
    "s_and_b32 s74, s31, s29\n"
    "s_andn2_b32 s74, s74, s30\n"
    "s_mov_b32 s31, s82\n"
    "s_cmp_eq_u32 s74, 0x7f\n"
    "s_cbranch_scc1 L_got_%=\n"
    "s_cmp_lt_u32 s91, 200000\n"
    "s_cbranch_scc1 L_poll_%=\n"
    "L_got_%=:\n"
    "s_cmp_lg_u32 s83, 0\n"
    "s_lshl_b32 s74, s27, 7\n"
    "s_lshl_b32 s75, s60, 15\n"
    "s_add_u32 s74, s74, s75\n"
    "s_add_u32 s75, s74, 16384\n"
    "s_waitcnt vmcnt(0)\n"
    "v_mfma_f32_16x16x32_bf16 v[48:51], v[208:211], v[152:155], v[48:51]\n"
    "s_waitcnt vmcnt(0)\n"
    "v_mfma_f32_16x16x32_bf16 v[48:51], v[208:211], v[160:163], v[48:51]\n"
    "s_waitcnt vmcnt(0)\n"
    "v_mfma_f32_16x16x32_bf16 v[48:51], v[208:211], v[168:171], v[48:51]\n"
    "s_waitcnt vmcnt(0)\n"
    "v_mfma_f32_16x16x32_bf16 v[48:51], v[208:211], v[176:179], v[48:51]\n"
    "s_waitcnt vmcnt(0)\n"
    "v_mfma_f32_16x16x32_bf16 v[48:51], v[208:211], v[184:187], v[48:51]\n"
    "s_waitcnt vmcnt(0)\n"
    "v_mfma_f32_16x16x32_bf16 v[48:51], v[208:211], v[192:195], v[48:51]\n"
    "s_waitcnt vmcnt(0)\n"
    "v_mfma_f32_16x16x32_bf16 v[48:51], v[208:211], v[200:203], v[48:51]\n"
    "v_mfma_f32_16x16x32_bf16 v[52:55], v[212:215], v[152:155], v[52:55]\n"
    "v_mfma_f32_16x16x32_bf16 v[52:55], v[212:215], v[160:163], v[52:55]\n"
    "s_nop 5\n"
    "v_cvt_pk_bf16_f32 v96, v48, v49\n"
    "v_mfma_f32_16x16x32_bf16 v[52:55], v[212:215], v[168:171], v[52:55]\n"
    "v_cvt_pk_bf16_f32 v97, v50, v51\n"
    "v_mfma_f32_16x16x32_bf16 v[52:55], v[212:215], v[176:179], v[52:55]\n"
    "v_mfma_f32_16x16x32_bf16 v[52:55], v[212:215], v[184:187], v[52:55]\n"
    "v_mfma_f32_16x16x32_bf16 v[52:55], v[212:215], v[192:195], v[52:55]\n"
    "v_mfma_f32_16x16x32_bf16 v[52:55], v[212:215], v[200:203], v[52:55]\n"
    "v_mfma_f32_16x16x32_bf16 v[56:59], v[208:211], v[156:159], v[56:59]\n"
    "v_mfma_f32_16x16x32_bf16 v[56:59], v[208:211], v[164:167], v[56:59]\n"
    "s_nop 5\n"
    "v_cvt_pk_bf16_f32 v98, v52, v53\n"
    "v_mfma_f32_16x16x32_bf16 v[56:59], v[208:211], v[172:175], v[56:59]\n"
    "v_cvt_pk_bf16_f32 v99, v54, v55\n"
    "v_permlane16_swap_b32 v96, v98\n"
    "v_mfma_f32_16x16x32_bf16 v[56:59], v[208:211], v[180:183], v[56:59]\n"
    "v_permlane16_swap_b32 v97, v99\n"
    "v_mfma_f32_16x16x32_bf16 v[56:59], v[208:211], v[188:191], v[56:59]\n"
    "ds_write_b128 v27, v[96:99] offset:0\n"
    "v_mfma_f32_16x16x32_bf16 v[56:59], v[208:211], v[196:199], v[56:59]\n"
    "s_waitcnt vmcnt(0)\n"
    "v_mfma_f32_16x16x32_bf16 v[56:59], v[208:211], v[204:207], v[56:59]\n"
    "v_mfma_f32_16x16x32_bf16 v[60:63], v[212:215], v[156:159], v[60:63]\n"
    "v_mfma_f32_16x16x32_bf16 v[60:63], v[212:215], v[164:167], v[60:63]\n"
    "s_nop 5\n"
    "v_cvt_pk_bf16_f32 v100, v56, v57\n"
    "v_mfma_f32_16x16x32_bf16 v[60:63], v[212:215], v[172:175], v[60:63]\n"
    "v_cvt_pk_bf16_f32 v101, v58, v59\n"
    "v_mfma_f32_16x16x32_bf16 v[60:63], v[212:215], v[180:183], v[60:63]\n"
    "v_mfma_f32_16x16x32_bf16 v[60:63], v[212:215], v[188:191], v[60:63]\n"
    "v_mfma_f32_16x16x32_bf16 v[60:63], v[212:215], v[196:199], v[60:63]\n"
    "v_mfma_f32_16x16x32_bf16 v[60:63], v[212:215], v[204:207], v[60:63]\n"
    "s_nop 7\n"
    "v_cvt_pk_bf16_f32 v102, v60, v61\n"
    "v_cvt_pk_bf16_f32 v103, v62, v63\n"
    "s_nop 0\n"
    "v_permlane16_swap_b32 v100, v102\n"
    "v_permlane16_swap_b32 v101, v103\n"
    "ds_write_b128 v27, v[100:103] offset:4096\n"
    "s_waitcnt lgkmcnt(0)\n"
    "s_barrier\n"
    "ds_read_b128 v[96:99], v220\n"
    "ds_read_b128 v[100:103], v220 offset:1024\n"
    "s_waitcnt lgkmcnt(1)\n"
    "buffer_store_dwordx4 v[96:99], v221, s[56:59], s74 offen sc1\n"
    "s_waitcnt lgkmcnt(0)\n"
    "buffer_store_dwordx4 v[100:103], v221, s[56:59], s75 offen sc1\n"
    "s_endpgm\n"
    "L_o0_%=:\n"
    "s_mov_b32 s67, 0\n"
    "s_barrier\n"
    "s_and_b32 s74, s61, 1\n"
    "s_mul_i32 s74, s74, 2\n"
    "s_xor_b32 s76, s74, 4\n"
    "s_lshl_b32 s75, s76, 13\n"
    "s_add_u32 m0, s75, s90\n"
    "s_add_u32 s76, s76, s63\n"
    "s_and_b32 s76, s76, 7\n"
    "s_lshl_b32 s76, s76, 7\n"
    "buffer_load_dwordx4 v78, s[84:87], s76 offen lds\n"
    "buffer_load_dwordx4 v79, s[84:87], s76 offen offset:1024 lds\n"
    "s_xor_b32 s76, s74, 5\n"
    "s_lshl_b32 s75, s76, 13\n"
    "s_add_u32 m0, s75, s90\n"
    "s_add_u32 s76, s76, s63\n"
    "s_and_b32 s76, s76, 7\n"
    "s_lshl_b32 s76, s76, 7\n"
    "buffer_load_dwordx4 v78, s[84:87], s76 offen lds\n"
    "buffer_load_dwordx4 v79, s[84:87], s76 offen offset:1024 lds\n"
    "s_xor_b32 s76, s74, 6\n"
    "s_lshl_b32 s75, s76, 13\n"
    "s_add_u32 m0, s75, s90\n"
    "s_add_u32 s76, s76, s63\n"
    "s_and_b32 s76, s76, 7\n"
    "s_lshl_b32 s76, s76, 7\n"
    "buffer_load_dwordx4 v78, s[84:87], s76 offen lds\n"
    "buffer_load_dwordx4 v79, s[84:87], s76 offen offset:1024 lds\n"
    "s_xor_b32 s76, s74, 7\n"
    "s_lshl_b32 s75, s76, 13\n"
    "s_add_u32 m0, s75, s90\n"
    "s_add_u32 s76, s76, s63\n"
    "s_and_b32 s76, s76, 7\n"
    "s_lshl_b32 s76, s76, 7\n"
    "buffer_load_dwordx4 v78, s[84:87], s76 offen lds\n"
    "buffer_load_dwordx4 v79, s[84:87], s76 offen offset:1024 lds\n"
    "s_add_u32 m0, s66, 16384\n"
    "s_xor_b32 s74, s26, 0\n"
    "buffer_load_dwordx4 v66, s[40:43], s74 offen offset:0 lds\n"
    "buffer_load_dwordx4 v66, s[40:43], s74 offen offset:1024 lds\n"
    "buffer_load_dwordx4 v67, s[40:43], s74 offen offset:2048 lds\n"
    "buffer_load_dwordx4 v67, s[40:43], s74 offen offset:3072 lds\n"
    "s_add_u32 m0, s66, 20480\n"
    "s_xor_b32 s74, s26, 4096\n"
    "buffer_load_dwordx4 v68, s[40:43], s74 offen offset:0 lds\n"
    "buffer_load_dwordx4 v68, s[40:43], s74 offen offset:1024 lds\n"
    "buffer_load_dwordx4 v69, s[40:43], s74 offen offset:2048 lds\n"
    "buffer_load_dwordx4 v69, s[40:43], s74 offen offset:3072 lds\n"
    "s_branch L_b0_%=\n"
    "L_o1_%=:\n"
    "s_mov_b32 s67, 1\n"
    "s_barrier\n"
    "s_and_b32 s74, s61, 1\n"
    "s_mul_i32 s74, s74, 2\n"
    "s_xor_b32 s76, s74, 4\n"
    "s_lshl_b32 s75, s76, 13\n"
    "s_add_u32 m0, s75, s90\n"
    "s_add_u32 s76, s76, s63\n"
    "s_and_b32 s76, s76, 7\n"
    "s_lshl_b32 s76, s76, 7\n"
    "buffer_load_dwordx4 v78, s[84:87], s76 offen lds\n"
    "buffer_load_dwordx4 v79, s[84:87], s76 offen offset:1024 lds\n"
    "s_xor_b32 s76, s74, 5\n"
    "s_lshl_b32 s75, s76, 13\n"
    "s_add_u32 m0, s75, s90\n"
    "s_add_u32 s76, s76, s63\n"
    "s_and_b32 s76, s76, 7\n"
    "s_lshl_b32 s76, s76, 7\n"
    "buffer_load_dwordx4 v78, s[84:87], s76 offen lds\n"
    "buffer_load_dwordx4 v79, s[84:87], s76 offen offset:1024 lds\n"
    "s_xor_b32 s76, s74, 6\n"
    "s_lshl_b32 s75, s76, 13\n"
    "s_add_u32 m0, s75, s90\n"
    "s_add_u32 s76, s76, s63\n"
    "s_and_b32 s76, s76, 7\n"
    "s_lshl_b32 s76, s76, 7\n"
    "buffer_load_dwordx4 v78, s[84:87], s76 offen lds\n"
    "buffer_load_dwordx4 v79, s[84:87], s76 offen offset:1024 lds\n"
    "s_xor_b32 s76, s74, 7\n"
    "s_lshl_b32 s75, s76, 13\n"
    "s_add_u32 m0, s75, s90\n"
    "s_add_u32 s76, s76, s63\n"
    "s_and_b32 s76, s76, 7\n"
    "s_lshl_b32 s76, s76, 7\n"
    "buffer_load_dwordx4 v78, s[84:87], s76 offen lds\n"
    "buffer_load_dwordx4 v79, s[84:87], s76 offen offset:1024 lds\n"
    "s_add_u32 m0, s66, 16384\n"
    "s_xor_b32 s74, s26, 0\n"
    "buffer_load_dwordx4 v66, s[40:43], s74 offen offset:0 lds\n"
    "buffer_load_dwordx4 v66, s[40:43], s74 offen offset:1024 lds\n"
    "buffer_load_dwordx4 v67, s[40:43], s74 offen offset:2048 lds\n"
    "buffer_load_dwordx4 v67, s[40:43], s74 offen offset:3072 lds\n"
    "s_add_u32 m0, s66, 20480\n"
    "s_xor_b32 s74, s26, 4096\n"
    "buffer_load_dwordx4 v68, s[40:43], s74 offen offset:0 lds\n"
    "buffer_load_dwordx4 v68, s[40:43], s74 offen offset:1024 lds\n"
    "buffer_load_dwordx4 v69, s[40:43], s74 offen offset:2048 lds\n"
    "buffer_load_dwordx4 v69, s[40:43], s74 offen offset:3072 lds\n"
    "s_branch L_b1_%=\n"
    "L_o2_%=:\n"
    "s_mov_b32 s67, 2\n"
    "s_barrier\n"
    "s_and_b32 s74, s61, 1\n"
    "s_mul_i32 s74, s74, 2\n"
    "s_xor_b32 s76, s74, 4\n"
    "s_lshl_b32 s75, s76, 13\n"
    "s_add_u32 m0, s75, s90\n"
    "s_add_u32 s76, s76, s63\n"
    "s_and_b32 s76, s76, 7\n"
    "s_lshl_b32 s76, s76, 7\n"
    "buffer_load_dwordx4 v78, s[84:87], s76 offen lds\n"
    "buffer_load_dwordx4 v79, s[84:87], s76 offen offset:1024 lds\n"
    "s_xor_b32 s76, s74, 5\n"
    "s_lshl_b32 s75, s76, 13\n"
    "s_add_u32 m0, s75, s90\n"
    "s_add_u32 s76, s76, s63\n"
    "s_and_b32 s76, s76, 7\n"
    "s_lshl_b32 s76, s76, 7\n"
    "buffer_load_dwordx4 v78, s[84:87], s76 offen lds\n"
    "buffer_load_dwordx4 v79, s[84:87], s76 offen offset:1024 lds\n"
    "s_xor_b32 s76, s74, 6\n"
    "s_lshl_b32 s75, s76, 13\n"
    "s_add_u32 m0, s75, s90\n"
    "s_add_u32 s76, s76, s63\n"
    "s_and_b32 s76, s76, 7\n"
    "s_lshl_b32 s76, s76, 7\n"
    "buffer_load_dwordx4 v78, s[84:87], s76 offen lds\n"
    "buffer_load_dwordx4 v79, s[84:87], s76 offen offset:1024 lds\n"
    "s_xor_b32 s76, s74, 7\n"
    "s_lshl_b32 s75, s76, 13\n"
    "s_add_u32 m0, s75, s90\n"
    "s_add_u32 s76, s76, s63\n"
    "s_and_b32 s76, s76, 7\n"
    "s_lshl_b32 s76, s76, 7\n"
    "buffer_load_dwordx4 v78, s[84:87], s76 offen lds\n"
    "buffer_load_dwordx4 v79, s[84:87], s76 offen offset:1024 lds\n"
    "s_add_u32 m0, s66, 16384\n"
    "s_xor_b32 s74, s26, 0\n"
    "buffer_load_dwordx4 v66, s[40:43], s74 offen offset:0 lds\n"
    "buffer_load_dwordx4 v66, s[40:43], s74 offen offset:1024 lds\n"
    "buffer_load_dwordx4 v67, s[40:43], s74 offen offset:2048 lds\n"
    "buffer_load_dwordx4 v67, s[40:43], s74 offen offset:3072 lds\n"
    "s_add_u32 m0, s66, 20480\n"
    "s_xor_b32 s74, s26, 4096\n"
    "buffer_load_dwordx4 v68, s[40:43], s74 offen offset:0 lds\n"
    "buffer_load_dwordx4 v68, s[40:43], s74 offen offset:1024 lds\n"
    "buffer_load_dwordx4 v69, s[40:43], s74 offen offset:2048 lds\n"
    "buffer_load_dwordx4 v69, s[40:43], s74 offen offset:3072 lds\n"
    "s_branch L_b2_%=\n"
    "L_o3_%=:\n"
    "s_mov_b32 s67, 3\n"
    "s_barrier\n"
    "s_and_b32 s74, s61, 1\n"
    "s_mul_i32 s74, s74, 2\n"
    "s_xor_b32 s76, s74, 4\n"
    "s_lshl_b32 s75, s76, 13\n"
    "s_add_u32 m0, s75, s90\n"
    "s_add_u32 s76, s76, s63\n"
    "s_and_b32 s76, s76, 7\n"
    "s_lshl_b32 s76, s76, 7\n"
    "buffer_load_dwordx4 v78, s[84:87], s76 offen lds\n"
    "buffer_load_dwordx4 v79, s[84:87], s76 offen offset:1024 lds\n"
    "s_xor_b32 s76, s74, 5\n"
    "s_lshl_b32 s75, s76, 13\n"
    "s_add_u32 m0, s75, s90\n"
    "s_add_u32 s76, s76, s63\n"
    "s_and_b32 s76, s76, 7\n"
    "s_lshl_b32 s76, s76, 7\n"
    "buffer_load_dwordx4 v78, s[84:87], s76 offen lds\n"
    "buffer_load_dwordx4 v79, s[84:87], s76 offen offset:1024 lds\n"
    "s_xor_b32 s76, s74, 6\n"
    "s_lshl_b32 s75, s76, 13\n"
    "s_add_u32 m0, s75, s90\n"
    "s_add_u32 s76, s76, s63\n"
    "s_and_b32 s76, s76, 7\n"
    "s_lshl_b32 s76, s76, 7\n"
    "buffer_load_dwordx4 v78, s[84:87], s76 offen lds\n"
    "buffer_load_dwordx4 v79, s[84:87], s76 offen offset:1024 lds\n"
    "s_xor_b32 s76, s74, 7\n"
    "s_lshl_b32 s75, s76, 13\n"
    "s_add_u32 m0, s75, s90\n"
    "s_add_u32 s76, s76, s63\n"
    "s_and_b32 s76, s76, 7\n"
    "s_lshl_b32 s76, s76, 7\n"
    "buffer_load_dwordx4 v78, s[84:87], s76 offen lds\n"
    "buffer_load_dwordx4 v79, s[84:87], s76 offen offset:1024 lds\n"
    "s_add_u32 m0, s66, 16384\n"
    "s_xor_b32 s74, s26, 0\n"
    "buffer_load_dwordx4 v66, s[40:43], s74 offen offset:0 lds\n"
    "buffer_load_dwordx4 v66, s[40:43], s74 offen offset:1024 lds\n"
    "buffer_load_dwordx4 v67, s[40:43], s74 offen offset:2048 lds\n"
    "buffer_load_dwordx4 v67, s[40:43], s74 offen offset:3072 lds\n"
    "s_add_u32 m0, s66, 20480\n"
    "s_xor_b32 s74, s26, 4096\n"
    "buffer_load_dwordx4 v68, s[40:43], s74 offen offset:0 lds\n"
    "buffer_load_dwordx4 v68, s[40:43], s74 offen offset:1024 lds\n"
    "buffer_load_dwordx4 v69, s[40:43], s74 offen offset:2048 lds\n"
    "buffer_load_dwordx4 v69, s[40:43], s74 offen offset:3072 lds\n"
    "s_branch L_b3_%=\n"
    "L_o4_%=:\n"
    "s_mov_b32 s67, 4\n"
    "s_waitcnt vmcnt(8)\n"
    "ds_read_b64 a[128:129], v70 offset:0\n"
    "ds_read_b64 a[130:131], v71 offset:0\n"
    "ds_read_b64 a[132:133], v72 offset:0\n"
    "ds_read_b64 a[134:135], v73 offset:0\n"
    "ds_read_b64 a[136:137], v74 offset:0\n"
    "ds_read_b64 a[138:139], v75 offset:0\n"
    "ds_read_b64 a[140:141], v76 offset:0\n"
    "ds_read_b64 a[142:143], v77 offset:0\n"
    "ds_read_b64 a[144:145], v70 offset:1024\n"
    "ds_read_b64 a[146:147], v71 offset:1024\n"
    "ds_read_b64 a[148:149], v72 offset:1024\n"
    "ds_read_b64 a[150:151], v73 offset:1024\n"
    "ds_read_b64 a[152:153], v74 offset:1024\n"
    "ds_read_b64 a[154:155], v75 offset:1024\n"
    "ds_read_b64 a[156:157], v76 offset:1024\n"
    "ds_read_b64 a[158:159], v77 offset:1024\n"
    "s_waitcnt lgkmcnt(0)\n"
    "s_barrier\n"
    "s_add_u32 m0, s66, 16384\n"
    "s_xor_b32 s74, s26, 8192\n"
    "buffer_load_dwordx4 v66, s[40:43], s74 offen offset:0 lds\n"
    "buffer_load_dwordx4 v66, s[40:43], s74 offen offset:1024 lds\n"
    "buffer_load_dwordx4 v67, s[40:43], s74 offen offset:2048 lds\n"
    "buffer_load_dwordx4 v67, s[40:43], s74 offen offset:3072 lds\n"
    "s_add_u32 m0, s66, 20480\n"
    "s_xor_b32 s74, s26, 12288\n"
    "buffer_load_dwordx4 v68, s[40:43], s74 offen offset:0 lds\n"
    "buffer_load_dwordx4 v68, s[40:43], s74 offen offset:1024 lds\n"
    "buffer_load_dwordx4 v69, s[40:43], s74 offen offset:2048 lds\n"
    "buffer_load_dwordx4 v69, s[40:43], s74 offen offset:3072 lds\n"
    "s_branch L_b4_%=\n"
    "L_o5_%=:\n"
    "s_mov_b32 s67, 5\n"
    "ds_read_b64 a[128:129], v70 offset:0\n"
    "ds_read_b64 a[130:131], v71 offset:0\n"
    "ds_read_b64 a[132:133], v72 offset:0\n"
    "ds_read_b64 a[134:135], v73 offset:0\n"
    "ds_read_b64 a[136:137], v74 offset:0\n"
    "ds_read_b64 a[138:139], v75 offset:0\n"
    "ds_read_b64 a[140:141], v76 offset:0\n"
    "ds_read_b64 a[142:143], v77 offset:0\n"
    "ds_read_b64 a[144:145], v70 offset:1024\n"
    "ds_read_b64 a[146:147], v71 offset:1024\n"
    "ds_read_b64 a[148:149], v72 offset:1024\n"
    "ds_read_b64 a[150:151], v73 offset:1024\n"
    "ds_read_b64 a[152:153], v74 offset:1024\n"
    "ds_read_b64 a[154:155], v75 offset:1024\n"
    "ds_read_b64 a[156:157], v76 offset:1024\n"
    "ds_read_b64 a[158:159], v77 offset:1024\n"
    "s_waitcnt lgkmcnt(0)\n"
    "s_barrier\n"
    "s_add_u32 m0, s66, 16384\n"
    "s_xor_b32 s74, s26, 8192\n"
    "buffer_load_dwordx4 v66, s[40:43], s74 offen offset:0 lds\n"
    "buffer_load_dwordx4 v66, s[40:43], s74 offen offset:1024 lds\n"
    "buffer_load_dwordx4 v67, s[40:43], s74 offen offset:2048 lds\n"
    "buffer_load_dwordx4 v67, s[40:43], s74 offen offset:3072 lds\n"
    "s_add_u32 m0, s66, 20480\n"
    "s_xor_b32 s74, s26, 12288\n"
    "buffer_load_dwordx4 v68, s[40:43], s74 offen offset:0 lds\n"
    "buffer_load_dwordx4 v68, s[40:43], s74 offen offset:1024 lds\n"
    "buffer_load_dwordx4 v69, s[40:43], s74 offen offset:2048 lds\n"
    "buffer_load_dwordx4 v69, s[40:43], s74 offen offset:3072 lds\n"
    "s_branch L_b5_%=\n"
    "L_o6_%=:\n"
    "s_mov_b32 s67, 6\n"
    "ds_read_b64 a[128:129], v70 offset:0\n"
    "ds_read_b64 a[130:131], v71 offset:0\n"
    "ds_read_b64 a[132:133], v72 offset:0\n"
    "ds_read_b64 a[134:135], v73 offset:0\n"
    "ds_read_b64 a[136:137], v74 offset:0\n"
    "ds_read_b64 a[138:139], v75 offset:0\n"
    "ds_read_b64 a[140:141], v76 offset:0\n"
    "ds_read_b64 a[142:143], v77 offset:0\n"
    "ds_read_b64 a[144:145], v70 offset:1024\n"
    "ds_read_b64 a[146:147], v71 offset:1024\n"
    "ds_read_b64 a[148:149], v72 offset:1024\n"
    "ds_read_b64 a[150:151], v73 offset:1024\n"
    "ds_read_b64 a[152:153], v74 offset:1024\n"
    "ds_read_b64 a[154:155], v75 offset:1024\n"
    "ds_read_b64 a[156:157], v76 offset:1024\n"
    "ds_read_b64 a[158:159], v77 offset:1024\n"
    "s_waitcnt lgkmcnt(0)\n"
    "s_barrier\n"
    "s_add_u32 m0, s66, 16384\n"
    "s_xor_b32 s74, s26, 8192\n"
    "buffer_load_dwordx4 v66, s[40:43], s74 offen offset:0 lds\n"
    "buffer_load_dwordx4 v66, s[40:43], s74 offen offset:1024 lds\n"
    "buffer_load_dwordx4 v67, s[40:43], s74 offen offset:2048 lds\n"
    "buffer_load_dwordx4 v67, s[40:43], s74 offen offset:3072 lds\n"
    "s_add_u32 m0, s66, 20480\n"
    "s_xor_b32 s74, s26, 12288\n"
    "buffer_load_dwordx4 v68, s[40:43], s74 offen offset:0 lds\n"
    "buffer_load_dwordx4 v68, s[40:43], s74 offen offset:1024 lds\n"
    "buffer_load_dwordx4 v69, s[40:43], s74 offen offset:2048 lds\n"
    "buffer_load_dwordx4 v69, s[40:43], s74 offen offset:3072 lds\n"
    "s_branch L_b6_%=\n"
    "L_o7_%=:\n"
    "s_mov_b32 s67, 7\n"
    "ds_read_b64 a[128:129], v70 offset:0\n"
    "ds_read_b64 a[130:131], v71 offset:0\n"
    "ds_read_b64 a[132:133], v72 offset:0\n"
    "ds_read_b64 a[134:135], v73 offset:0\n"
    "ds_read_b64 a[136:137], v74 offset:0\n"
    "ds_read_b64 a[138:139], v75 offset:0\n"
    "ds_read_b64 a[140:141], v76 offset:0\n"
    "ds_read_b64 a[142:143], v77 offset:0\n"
    "ds_read_b64 a[144:145], v70 offset:1024\n"
    "ds_read_b64 a[146:147], v71 offset:1024\n"
    "ds_read_b64 a[148:149], v72 offset:1024\n"
    "ds_read_b64 a[150:151], v73 offset:1024\n"
    "ds_read_b64 a[152:153], v74 offset:1024\n"
    "ds_read_b64 a[154:155], v75 offset:1024\n"
    "ds_read_b64 a[156:157], v76 offset:1024\n"
    "ds_read_b64 a[158:159], v77 offset:1024\n"
    "s_waitcnt lgkmcnt(0)\n"
    "s_barrier\n"
    "s_add_u32 m0, s66, 16384\n"
    "s_xor_b32 s74, s26, 8192\n"
    "buffer_load_dwordx4 v66, s[40:43], s74 offen offset:0 lds\n"
    "buffer_load_dwordx4 v66, s[40:43], s74 offen offset:1024 lds\n"
    "buffer_load_dwordx4 v67, s[40:43], s74 offen offset:2048 lds\n"
    "buffer_load_dwordx4 v67, s[40:43], s74 offen offset:3072 lds\n"
    "s_add_u32 m0, s66, 20480\n"
    "s_xor_b32 s74, s26, 12288\n"
    "buffer_load_dwordx4 v68, s[40:43], s74 offen offset:0 lds\n"
    "buffer_load_dwordx4 v68, s[40:43], s74 offen offset:1024 lds\n"
    "buffer_load_dwordx4 v69, s[40:43], s74 offen offset:2048 lds\n"
    "buffer_load_dwordx4 v69, s[40:43], s74 offen offset:3072 lds\n"
    "s_branch L_b7_%=\n"
    "L_o8_%=:\n"
    "s_mov_b32 s67, 8\n"
    "s_waitcnt vmcnt(8)\n"
    "ds_read_b64 a[160:161], v70 offset:0\n"
    "ds_read_b64 a[162:163], v71 offset:0\n"
    "ds_read_b64 a[164:165], v72 offset:0\n"
    "ds_read_b64 a[166:167], v73 offset:0\n"
    "ds_read_b64 a[168:169], v74 offset:0\n"
    "ds_read_b64 a[170:171], v75 offset:0\n"
    "ds_read_b64 a[172:173], v76 offset:0\n"
    "ds_read_b64 a[174:175], v77 offset:0\n"
    "ds_read_b64 a[176:177], v70 offset:1024\n"
    "ds_read_b64 a[178:179], v71 offset:1024\n"
    "ds_read_b64 a[180:181], v72 offset:1024\n"
    "ds_read_b64 a[182:183], v73 offset:1024\n"
    "ds_read_b64 a[184:185], v74 offset:1024\n"
    "ds_read_b64 a[186:187], v75 offset:1024\n"
    "ds_read_b64 a[188:189], v76 offset:1024\n"
    "ds_read_b64 a[190:191], v77 offset:1024\n"
    "s_waitcnt lgkmcnt(0)\n"
    "s_barrier\n"
    "s_add_u32 m0, s66, 16384\n"
    "s_xor_b32 s74, s26, 0\n"
    "buffer_load_dwordx4 v64, s[44:47], s74 offen offset:0 lds\n"
    "s_xor_b32 s74, s26, 1024\n"
    "buffer_load_dwordx4 v65, s[44:47], s74 offen offset:1024 lds\n"
    "s_xor_b32 s74, s26, 2048\n"
    "buffer_load_dwordx4 v64, s[44:47], s74 offen offset:2048 lds\n"
    "s_xor_b32 s74, s26, 3072\n"
    "buffer_load_dwordx4 v65, s[44:47], s74 offen offset:3072 lds\n"
    "s_add_u32 m0, s66, 20480\n"
    "s_xor_b32 s74, s26, 128\n"
    "buffer_load_dwordx4 v64, s[44:47], s74 offen offset:0 lds\n"
    "s_xor_b32 s74, s26, 1152\n"
    "buffer_load_dwordx4 v65, s[44:47], s74 offen offset:1024 lds\n"
    "s_xor_b32 s74, s26, 2176\n"
    "buffer_load_dwordx4 v64, s[44:47], s74 offen offset:2048 lds\n"
    "s_xor_b32 s74, s26, 3200\n"
    "buffer_load_dwordx4 v65, s[44:47], s74 offen offset:3072 lds\n"
    "s_branch L_b8_%=\n"
    "L_o9_%=:\n"
    "s_mov_b32 s67, 9\n"
    "ds_read_b64 a[160:161], v70 offset:0\n"
    "ds_read_b64 a[162:163], v71 offset:0\n"
    "ds_read_b64 a[164:165], v72 offset:0\n"
    "ds_read_b64 a[166:167], v73 offset:0\n"
    "ds_read_b64 a[168:169], v74 offset:0\n"
    "ds_read_b64 a[170:171], v75 offset:0\n"
    "ds_read_b64 a[172:173], v76 offset:0\n"
    "ds_read_b64 a[174:175], v77 offset:0\n"
    "ds_read_b64 a[176:177], v70 offset:1024\n"
    "ds_read_b64 a[178:179], v71 offset:1024\n"
    "ds_read_b64 a[180:181], v72 offset:1024\n"
    "ds_read_b64 a[182:183], v73 offset:1024\n"
    "ds_read_b64 a[184:185], v74 offset:1024\n"
    "ds_read_b64 a[186:187], v75 offset:1024\n"
    "ds_read_b64 a[188:189], v76 offset:1024\n"
    "ds_read_b64 a[190:191], v77 offset:1024\n"
    "s_waitcnt lgkmcnt(0)\n"
    "s_barrier\n"
    "s_add_u32 m0, s66, 16384\n"
    "s_xor_b32 s74, s26, 0\n"
    "buffer_load_dwordx4 v64, s[44:47], s74 offen offset:0 lds\n"
    "s_xor_b32 s74, s26, 1024\n"
    "buffer_load_dwordx4 v65, s[44:47], s74 offen offset:1024 lds\n"
    "s_xor_b32 s74, s26, 2048\n"
    "buffer_load_dwordx4 v64, s[44:47], s74 offen offset:2048 lds\n"
    "s_xor_b32 s74, s26, 3072\n"
    "buffer_load_dwordx4 v65, s[44:47], s74 offen offset:3072 lds\n"
    "s_add_u32 m0, s66, 20480\n"
    "s_xor_b32 s74, s26, 128\n"
    "buffer_load_dwordx4 v64, s[44:47], s74 offen offset:0 lds\n"
    "s_xor_b32 s74, s26, 1152\n"
    "buffer_load_dwordx4 v65, s[44:47], s74 offen offset:1024 lds\n"
    "s_xor_b32 s74, s26, 2176\n"
    "buffer_load_dwordx4 v64, s[44:47], s74 offen offset:2048 lds\n"
    "s_xor_b32 s74, s26, 3200\n"
    "buffer_load_dwordx4 v65, s[44:47], s74 offen offset:3072 lds\n"
    "s_branch L_b9_%=\n"
    "L_o10_%=:\n"
    "s_mov_b32 s67, 10\n"
    "ds_read_b64 a[160:161], v70 offset:0\n"
    "ds_read_b64 a[162:163], v71 offset:0\n"
    "ds_read_b64 a[164:165], v72 offset:0\n"
    "ds_read_b64 a[166:167], v73 offset:0\n"
    "ds_read_b64 a[168:169], v74 offset:0\n"
    "ds_read_b64 a[170:171], v75 offset:0\n"
    "ds_read_b64 a[172:173], v76 offset:0\n"
    "ds_read_b64 a[174:175], v77 offset:0\n"
    "ds_read_b64 a[176:177], v70 offset:1024\n"
    "ds_read_b64 a[178:179], v71 offset:1024\n"
    "ds_read_b64 a[180:181], v72 offset:1024\n"
    "ds_read_b64 a[182:183], v73 offset:1024\n"
    "ds_read_b64 a[184:185], v74 offset:1024\n"
    "ds_read_b64 a[186:187], v75 offset:1024\n"
    "ds_read_b64 a[188:189], v76 offset:1024\n"
    "ds_read_b64 a[190:191], v77 offset:1024\n"
    "s_waitcnt lgkmcnt(0)\n"
    "s_barrier\n"
    "s_add_u32 m0, s66, 16384\n"
    "s_xor_b32 s74, s26, 0\n"
    "buffer_load_dwordx4 v64, s[44:47], s74 offen offset:0 lds\n"
    "s_xor_b32 s74, s26, 1024\n"
    "buffer_load_dwordx4 v65, s[44:47], s74 offen offset:1024 lds\n"
    "s_xor_b32 s74, s26, 2048\n"
    "buffer_load_dwordx4 v64, s[44:47], s74 offen offset:2048 lds\n"
    "s_xor_b32 s74, s26, 3072\n"
    "buffer_load_dwordx4 v65, s[44:47], s74 offen offset:3072 lds\n"
    "s_add_u32 m0, s66, 20480\n"
    "s_xor_b32 s74, s26, 128\n"
    "buffer_load_dwordx4 v64, s[44:47], s74 offen offset:0 lds\n"
    "s_xor_b32 s74, s26, 1152\n"
    "buffer_load_dwordx4 v65, s[44:47], s74 offen offset:1024 lds\n"
    "s_xor_b32 s74, s26, 2176\n"
    "buffer_load_dwordx4 v64, s[44:47], s74 offen offset:2048 lds\n"
    "s_xor_b32 s74, s26, 3200\n"
    "buffer_load_dwordx4 v65, s[44:47], s74 offen offset:3072 lds\n"
    "s_branch L_b10_%=\n"
    "L_o11_%=:\n"
    "s_mov_b32 s67, 11\n"
    "ds_read_b64 a[160:161], v70 offset:0\n"
    "ds_read_b64 a[162:163], v71 offset:0\n"
    "ds_read_b64 a[164:165], v72 offset:0\n"
    "ds_read_b64 a[166:167], v73 offset:0\n"
    "ds_read_b64 a[168:169], v74 offset:0\n"
    "ds_read_b64 a[170:171], v75 offset:0\n"
    "ds_read_b64 a[172:173], v76 offset:0\n"
    "ds_read_b64 a[174:175], v77 offset:0\n"
    "ds_read_b64 a[176:177], v70 offset:1024\n"
    "ds_read_b64 a[178:179], v71 offset:1024\n"
    "ds_read_b64 a[180:181], v72 offset:1024\n"
    "ds_read_b64 a[182:183], v73 offset:1024\n"
    "ds_read_b64 a[184:185], v74 offset:1024\n"
    "ds_read_b64 a[186:187], v75 offset:1024\n"
    "ds_read_b64 a[188:189], v76 offset:1024\n"
    "ds_read_b64 a[190:191], v77 offset:1024\n"
    "s_waitcnt lgkmcnt(0)\n"
    "s_barrier\n"
    "s_add_u32 m0, s66, 16384\n"
    "s_xor_b32 s74, s26, 0\n"
    "buffer_load_dwordx4 v64, s[44:47], s74 offen offset:0 lds\n"
    "s_xor_b32 s74, s26, 1024\n"
    "buffer_load_dwordx4 v65, s[44:47], s74 offen offset:1024 lds\n"
    "s_xor_b32 s74, s26, 2048\n"
    "buffer_load_dwordx4 v64, s[44:47], s74 offen offset:2048 lds\n"
    "s_xor_b32 s74, s26, 3072\n"
    "buffer_load_dwordx4 v65, s[44:47], s74 offen offset:3072 lds\n"
    "s_add_u32 m0, s66, 20480\n"
    "s_xor_b32 s74, s26, 128\n"
    "buffer_load_dwordx4 v64, s[44:47], s74 offen offset:0 lds\n"
    "s_xor_b32 s74, s26, 1152\n"
    "buffer_load_dwordx4 v65, s[44:47], s74 offen offset:1024 lds\n"
    "s_xor_b32 s74, s26, 2176\n"
    "buffer_load_dwordx4 v64, s[44:47], s74 offen offset:2048 lds\n"
    "s_xor_b32 s74, s26, 3200\n"
    "buffer_load_dwordx4 v65, s[44:47], s74 offen offset:3072 lds\n"
    "s_branch L_b11_%=\n"
    "L_fin_%=:\n"
    : : "s"(__builtin_amdgcn_kernarg_segment_ptr()), "s"(blockIdx.x), "v"(threadIdx.x), "s"((unsigned)(size_t)smem)
    : "v1", "v2", "v3", "v4", "v5", "v6", "v7", "v8", "v9", "v10", "v11", "v12", "v13", "v14", "v15", "v16", "v17", "v18", "v19", "v20", "v21", "v22", "v23", "v24", "v25", "v26", "v27", "v28", "v29", "v30", "v31", "v32", "v33", "v34", "v35", "v36", "v37", "v38", "v39", "v40", "v41", "v42", "v43", "v44", "v45", "v46", "v47", "v48", "v49", "v50", "v51", "v52", "v53", "v54", "v55", "v56", "v57", "v58", "v59", "v60", "v61", "v62", "v63", "v64", "v65", "v66", "v67", "v68", "v69", "v70", "v71", "v72", "v73", "v74", "v75", "v76", "v77", "v78", "v79", "v80", "v81", "v82", "v83", "v84", "v85", "v86", "v87", "v88", "v89", "v90", "v91", "v92", "v93", "v94", "v95", "v96", "v97", "v98", "v99", "v100", "v101", "v102", "v103", "v104", "v105", "v106", "v107", "v108", "v109", "v110", "v111", "v112", "v113", "v114", "v115", "v116", "v117", "v118", "v119", "v120", "v121", "v122", "v123", "v124", "v125", "v126", "v127", "v128", "v129", "v130", "v131", "v132", "v133", "v134", "v135", "v136", "v137", "v138", "v139", "v140", "v141", "v142", "v143", "v144", "v145", "v146", "v147", "v148", "v149", "v150", "v151", "v152", "v153", "v154", "v155", "v156", "v157", "v158", "v159", "v160", "v161", "v162", "v163", "v164", "v165", "v166", "v167", "v168", "v169", "v170", "v171", "v172", "v173", "v174", "v175", "v176", "v177", "v178", "v179", "v180", "v181", "v182", "v183", "v184", "v185", "v186", "v187", "v188", "v189", "v190", "v191", "v192", "v193", "v194", "v195", "v196", "v197", "v198", "v199", "v200", "v201", "v202", "v203", "v204", "v205", "v206", "v207", "v208", "v209", "v210", "v211", "v212", "v213", "v214", "v215", "v216", "v217", "v218", "v219", "v220", "v221", "v222", "v223", "v224", "v225", "v226", "v227", "v228", "v229", "v230", "v231", "v232", "v233", "v234", "v235", "v236", "v237", "v238", "v239", "v240", "v241", "v242", "v243", "v244", "v245", "v246", "v247", "v248", "v249", "v250", "v251", "v252", "v253", "v254", "v255", "a0", "a1", "a2", "a3", "a4", "a5", "a6", "a7", "a8", "a9", "a10", "a11", "a12", "a13", "a14", "a15", "a16", "a17", "a18", "a19", "a20", "a21", "a22", "a23", "a24", "a25", "a26", "a27", "a28", "a29", "a30", "a31", "a32", "a33", "a34", "a35", "a36", "a37", "a38", "a39", "a40", "a41", "a42", "a43", "a44", "a45", "a46", "a47", "a48", "a49", "a50", "a51", "a52", "a53", "a54", "a55", "a56", "a57", "a58", "a59", "a60", "a61", "a62", "a63", "a64", "a65", "a66", "a67", "a68", "a69", "a70", "a71", "a72", "a73", "a74", "a75", "a76", "a77", "a78", "a79", "a80", "a81", "a82", "a83", "a84", "a85", "a86", "a87", "a88", "a89", "a90", "a91", "a92", "a93", "a94", "a95", "a96", "a97", "a98", "a99", "a100", "a101", "a102", "a103", "a104", "a105", "a106", "a107", "a108", "a109", "a110", "a111", "a112", "a113", "a114", "a115", "a116", "a117", "a118", "a119", "a120", "a121", "a122", "a123", "a124", "a125", "a126", "a127", "a128", "a129", "a130", "a131", "a132", "a133", "a134", "a135", "a136", "a137", "a138", "a139", "a140", "a141", "a142", "a143", "a144", "a145", "a146", "a147", "a148", "a149", "a150", "a151", "a152", "a153", "a154", "a155", "a156", "a157", "a158", "a159", "a160", "a161", "a162", "a163", "a164", "a165", "a166", "a167", "a168", "a169", "a170", "a171", "a172", "a173", "a174", "a175", "a176", "a177", "a178", "a179", "a180", "a181", "a182", "a183", "a184", "a185", "a186", "a187", "a188", "a189", "a190", "a191", "a192", "a193", "a194", "a195", "a196", "a197", "a198", "a199", "a200", "a201", "a202", "a203", "a204", "a205", "a206", "a207", "a208", "a209", "a210", "a211", "a212", "a213", "a214", "a215", "a216", "a217", "a218", "a219", "a220", "a221", "a222", "a223", "a224", "a225", "a226", "a227", "a228", "a229", "a230", "a231", "a232", "a233", "a234", "a235", "a236", "a237", "a238", "a239", "a240", "a241", "a242", "a243", "a244", "a245", "a246", "a247", "a248", "a249", "a250", "a251", "a252", "a253", "a254", "a255", "s4", "s5", "s6", "s7", "s8", "s9", "s10", "s11", "s12", "s13", "s14", "s15", "s16", "s17", "s18", "s19", "s20", "s21", "s22", "s23", "s24", "s25", "s26", "s27", "s28", "s29", "s30", "s31", "s33", "s36", "s37", "s38", "s39", "s40", "s41", "s42", "s43", "s44", "s45", "s46", "s47", "s48", "s49", "s50", "s51", "s52", "s53", "s54", "s55", "s56", "s57", "s58", "s59", "s60", "s61", "s62", "s63", "s64", "s65", "s66", "s67", "s68", "s69", "s70", "s71", "s72", "s73", "s74", "s75", "s76", "s77", "s78", "s79", "s80", "s81", "s82", "s83", "s84", "s85", "s86", "s87", "s88", "s89", "s90", "s91", "s92", "s93", "s94", "s95", "s96", "s97", "s98", "s99", "vcc", "scc", "memory");
}

extern "C" void kernel_launch(void* const* d_in, const int* in_sizes, int n_in, void* d_out, int out_size, void* d_ws, size_t ws_size, hipStream_t stream) {
  Args a;
  for (int br = 0; br < 2; ++br) {
    a.br[br].x = (const char*)d_in[5*br+0]; a.br[br].k = (const char*)d_in[5*br+1]; a.br[br].v = (const char*)d_in[5*br+2];
    a.br[br].wq = (const char*)d_in[5*br+3]; a.br[br].wo = (const char*)d_in[5*br+4]; a.br[br].out = (char*)d_out + 1024 * br;
    a.br[br].ws = (char*)d_ws + 0u * 1048576u; a.br[br].ws2 = (char*)d_ws + (64u << 20); }
  k_fused<<<256, 256, 0, stream>>>(a);

}
```
